```python
import torch

def _softmax(x, axis=-1):
    return torch.softmax(x, dim=axis)
def _gelu(x):
    return torch.nn.functional.gelu(x, approximate="tanh")
def _silu(x):
    return torch.nn.functional.silu(x)
def _sigmoid(x):
    return torch.sigmoid(x)
def _layernorm(x, axis=-1, eps=1e-5):
    m = x.mean(dim=axis, keepdim=True)
    v = x.var(dim=axis, keepdim=True, unbiased=False)
    return (x - m) / torch.sqrt(v + eps)
def _scatter_add(tgt, idx, src, axis=0):
    return tgt.clone().index_add_(axis, idx, src)
def _segment_sum(src, seg, n):
    out = torch.zeros((n,) + src.shape[1:], dtype=src.dtype)
    return out.index_add_(0, seg, src)
def _pad(x, pw):
    flat = tuple(v for pair in reversed(pw) for v in pair)
    return torch.nn.functional.pad(x, flat)

kernel_name = 'parallel_sum__sliding_block_cached__mixer_channel_mlp__decode'

def setup_inputs():

    _w = (lambda a: (lambda t: t.to(torch.bfloat16) if t.is_floating_point() else t)(torch.as_tensor(a)))
    def _A_setup():
        B, Sq, D = 1, 1, 448
        Hq, Hkv, Sk, Dh = 16, 8, 8192, 256
        assert Hq % Hkv == 0, (Hq, Hkv)
        return {
            "x": _w((lambda *s: torch.randn(*s, dtype=torch.bfloat16) * 0.05)(B, Sq, D)),
            "k_cache": _w((lambda *s: torch.randn(*s, dtype=torch.bfloat16) * 3.2)(B, Hkv, Sk, Dh)),
            "v_cache": _w((lambda *s: torch.randn(*s, dtype=torch.bfloat16) * 0.4)(B, Hkv, Sk, Dh)),
            "wq": _w((lambda *s: torch.randn(*s, dtype=torch.bfloat16) * 3.2)(D, Hq * Dh) * (D ** -0.5)),
            "wo": _w((lambda *s: torch.randn(*s, dtype=torch.bfloat16) * 0.4)(Hq * Dh, D) * ((Hq * Dh) ** -0.5)),
        }
    _A_in = _A_setup()

    def _B_setup():
        B, S, D, F = 1, 1, 448, 1792
        return {
            "x": _w((lambda *s: torch.randn(*s, dtype=torch.bfloat16) * 0.05)(B, S, D)),
            "w1": _w((lambda *s: torch.randn(*s, dtype=torch.bfloat16) * 3.2)(D, F) * (D ** -0.5)),
            "b1": _w((lambda *s: torch.randn(*s, dtype=torch.bfloat16) * 1.6)(F)),
            "w2": _w((lambda *s: torch.randn(*s, dtype=torch.bfloat16) * 0.05)(F, D) * (F ** -0.5)),
            "b2": _w((lambda *s: torch.randn(*s, dtype=torch.bfloat16) * 0.05)(D)),
        }
    _B_in = _B_setup()

    return {
        "A_x": _A_in["x"],
        "A_k_cache": _A_in["k_cache"],
        "A_v_cache": _A_in["v_cache"],
        "A_wq": _A_in["wq"],
        "A_wo": _A_in["wo"],
        "B_x": _B_in["x"],
        "B_w1": _B_in["w1"],
        "B_b1": _B_in["b1"],
        "B_w2": _B_in["w2"],
        "B_b2": _B_in["b2"],
    }

def reference(A_x, A_k_cache, A_v_cache, A_wq, A_wo, B_x, B_w1, B_b1, B_w2, B_b2):
    def _A_ref(x, k_cache, v_cache, wq, wo):
        B, Sq, D = x.shape
        Hq, Dh = 16, 256
        Hkv, Sk = k_cache.shape[1], k_cache.shape[2]
        W = min(4096, max(256, Sk // 4))
        rep = Hq // Hkv
        q = (lambda x, *d: x.permute(*d))((x @ wq).reshape(B, Sq, Hq, Dh), 0, 2, 1, 3)
        k = (lambda x, n, axis: torch.repeat_interleave(x, n, dim=axis))(k_cache[:, :, Sk - W:, :], rep, axis=1)
        v = (lambda x, n, axis: torch.repeat_interleave(x, n, dim=axis))(v_cache[:, :, Sk - W:, :], rep, axis=1)
        scores = torch.einsum("bhqd,bhkd->bhqk", q, k) * (Dh ** -0.5)
        p = _softmax(scores, axis=-1)
        o = torch.einsum("bhqk,bhkd->bhqd", p, v)
        return (lambda x, *d: x.permute(*d))(o, 0, 2, 1, 3).reshape(B, Sq, Hq * Dh) @ wo

    def _B_ref(x, w1, b1, w2, b2):
        y = _layernorm(x, axis=-1)
        h = _gelu(torch.einsum("bsd,df->bsf", y, w1) + b1)
        return torch.einsum("bsf,fd->bsd", h, w2) + b2

    x = A_x
    xn = _layernorm(x, axis=-1)
    return x + _A_ref(xn, A_k_cache, A_v_cache, A_wq, A_wo) + _B_ref(xn, B_w1, B_b1, B_w2, B_b2)
```

```cpp
#include <hip/hip_runtime.h>
#include <hip/hip_bf16.h>
#define EMU 1
typedef unsigned short u16;
__device__ __forceinline__ float bf2f(u16 v) { return __uint_as_float(((unsigned)v) << 16); }
__device__ __forceinline__ float rb(float f) {
  unsigned u = __float_as_uint(f); u = (u + 0x7fffu + ((u >> 16) & 1u)) & 0xffff0000u; return __uint_as_float(u);
}
__device__ __forceinline__ u16 f2bf(float f) { unsigned u = __float_as_uint(f); return (u16)((u + 0x7fffu + ((u >> 16) & 1u)) >> 16); }
constexpr int D = 448, HQ = 16, HKV = 8, SK = 8192, DH = 256, W = 2048, F = 1792;

__device__ void ln_block(const float* in, float* out, float* red) {
  int t = threadIdx.x;
  float s = 0.f;
  for (int i = t; i < D; i += 256) s += in[i];
  red[t] = s; __syncthreads();
  for (int o = 128; o > 0; o >>= 1) { if (t < o) red[t] += red[t + o]; __syncthreads(); }
  float mean = red[0] / D; __syncthreads();
  s = 0.f;
  for (int i = t; i < D; i += 256) { float d = in[i] - mean; s += d * d; }
  red[t] = s; __syncthreads();
  for (int o = 128; o > 0; o >>= 1) { if (t < o) red[t] += red[t + o]; __syncthreads(); }
  float var = red[0] / D; __syncthreads();
  float m = rb(mean), v = rb(var);
  float den = rb(sqrtf(rb(v + 1e-5f)));
  for (int i = t; i < D; i += 256) out[i] = rb(rb(in[i] - m) / den);
  __syncthreads();
}

__global__ void k1(const u16* x, const u16* wq, const u16* w1, const u16* b1, float* q, float* h) {
  __shared__ float xs[D], xn[D], y[D], red[256];
  int t = threadIdx.x;
  for (int i = t; i < D; i += 256) xs[i] = bf2f(x[i]);
  __syncthreads();
  ln_block(xs, xn, red);
  ln_block(xn, y, red);
  int c = blockIdx.x * 256 + t;
  if (c < HQ * DH) {
    float a = 0.f;
    for (int i = 0; i < D; ++i) a += xn[i] * bf2f(wq[(size_t)i * HQ * DH + c]);
    q[c] = rb(a);
  } else {
    c -= HQ * DH;
    float a = 0.f;
    for (int i = 0; i < D; ++i) a += y[i] * bf2f(w1[(size_t)i * F + c]);
    a = rb(rb(a) + bf2f(b1[c]));
    float g = 0.5f * a * (1.f + tanhf(0.7978845608028654f * (a + 0.044715f * a * a * a)));
    h[c] = rb(g);
  }
}
__global__ void k2(const u16* kc, const u16* vc, const float* q, float* o) {
  __shared__ float qs[DH], p[W], red[256];
  int t = threadIdx.x, hd = blockIdx.x, g = hd / 2;
  qs[t] = q[hd * DH + t];
  __syncthreads();
  float mx = -INFINITY;
  for (int k = t; k < W; k += 256) {
    const u16* kr = kc + ((size_t)g * SK + (SK - W) + k) * DH;
    float a = 0.f;
    for (int d = 0; d < DH; ++d) a += qs[d] * bf2f(kr[d]);
    a = rb(a) * 0.0625f;
    p[k] = a; mx = fmaxf(mx, a);
  }
  red[t] = mx; __syncthreads();
  for (int s = 128; s > 0; s >>= 1) { if (t < s) red[t] = fmaxf(red[t], red[t + s]); __syncthreads(); }
  mx = red[0]; __syncthreads();
  float sum = 0.f;
  for (int k = t; k < W; k += 256) { float e = expf(p[k] - mx); p[k] = e; sum += e; }
  red[t] = sum; __syncthreads();
  for (int s = 128; s > 0; s >>= 1) { if (t < s) red[t] += red[t + s]; __syncthreads(); }
  sum = red[0]; __syncthreads();
  for (int k = t; k < W; k += 256) p[k] = rb(p[k] / sum);
  __syncthreads();
  float a = 0.f;
  for (int k = 0; k < W; ++k) a += p[k] * bf2f(vc[((size_t)g * SK + (SK - W) + k) * DH + t]);
  o[hd * DH + t] = rb(a);
}
__global__ void k3(const u16* x, const u16* wo, const u16* w2, const u16* b2, const float* o, const float* h, u16* out) {
  int c = blockIdx.x * 64 + threadIdx.x;
  float a = 0.f;
  for (int i = 0; i < HQ * DH; ++i) a += o[i] * bf2f(wo[(size_t)i * D + c]);
  float b = 0.f;
  for (int i = 0; i < F; ++i) b += h[i] * bf2f(w2[(size_t)i * D + c]);
  b = rb(rb(b) + bf2f(b2[c]));
  float r = rb(bf2f(x[c]) + rb(a));
  out[c] = f2bf(r + b);
}
extern "C" void kernel_launch(void* const* d_in, const int* in_sizes, int n_in, void* d_out, int out_size, void* d_ws, size_t ws_size, hipStream_t stream) {
  const u16* x = (const u16*)d_in[0]; const u16* kc = (const u16*)d_in[1]; const u16* vc = (const u16*)d_in[2];
  const u16* wq = (const u16*)d_in[3]; const u16* wo = (const u16*)d_in[4];
  const u16* w1 = (const u16*)d_in[6]; const u16* b1 = (const u16*)d_in[7]; const u16* w2 = (const u16*)d_in[8]; const u16* b2 = (const u16*)d_in[9];
  float* q = (float*)d_ws; float* h = q + 4096; float* o = h + 2048;
  k1<<<(4096 + 1792) / 256, 256, 0, stream>>>(x, wq, w1, b1, q, h);
  k2<<<16, 256, 0, stream>>>(kc, vc, q, o);
  k3<<<7, 64, 0, stream>>>(x, wo, w2, b2, o, h, (u16*)d_out);
}
```

```cpp
#include <hip/hip_runtime.h>
#include <hip/hip_bf16.h>
#include <stdint.h>

typedef unsigned short u16;
typedef unsigned u32x4 __attribute__((ext_vector_type(4)));
typedef unsigned u32x2 __attribute__((ext_vector_type(2)));
typedef float f32x4 __attribute__((ext_vector_type(4)));
typedef __bf16 bf16x2 __attribute__((ext_vector_type(2)));
#define LAS __attribute__((address_space(3)))
#define GAS __attribute__((address_space(1)))

constexpr int D = 448, HQ = 16, HKV = 8, SK = 8192, DH = 256, WIN = 2048, FF = 1792;
constexpr int NWG = 256, NWAVE = 12, NTHR = NWAVE * 64;
constexpr unsigned WS_EPOCH = 0;
constexpr unsigned WS_Q = 4096;
constexpr unsigned WS_ML = WS_Q + 8 * 2048;
constexpr unsigned WS_PO = WS_ML + 16 * 512;
constexpr unsigned WS_OUT = WS_PO + 256 * 4096;
constexpr unsigned WS_END = WS_OUT + 224 * 4096;
constexpr unsigned WS_STAMP = 8u << 20;
constexpr unsigned SPIN_CAP = 1u << 16;
#define AUX_STREAM 2
#define AUX_W 0
constexpr int AUX_SC1 = 16;

__device__ __forceinline__ float dot2(unsigned a, unsigned b, float c) {
  return __builtin_amdgcn_fdot2_f32_bf16(__builtin_bit_cast(bf16x2, a), __builtin_bit_cast(bf16x2, b), c, false);
}
__device__ __forceinline__ unsigned cvtpk(float lo, float hi) {
  bf16x2 r; r.x = (__bf16)lo; r.y = (__bf16)hi; return __builtin_bit_cast(unsigned, r);
}
__device__ __forceinline__ float rbf(float f) { return __uint_as_float(cvtpk(f, f) & 0xffff0000u); }
template <int CTRL> __device__ __forceinline__ float dpp(float x) {
  return __builtin_bit_cast(float, __builtin_amdgcn_mov_dpp(__builtin_bit_cast(int, x), CTRL, 0xf, 0xf, true));
}
constexpr int ROR1 = 0x121, ROR2 = 0x122, ROR4 = 0x124, ROR8 = 0x128;
__device__ __forceinline__ float swap16_add(float a, float b) {
  auto s = __builtin_amdgcn_permlane16_swap(__float_as_uint(a), __float_as_uint(b), false, false);
  return __uint_as_float(s[0]) + __uint_as_float(s[1]);
}
__device__ __forceinline__ float swap32_add(float a, float b) {
  auto s = __builtin_amdgcn_permlane32_swap(__float_as_uint(a), __float_as_uint(b), false, false);
  return __uint_as_float(s[0]) + __uint_as_float(s[1]);
}
__device__ __forceinline__ float swap16_max(float a) {
  auto s = __builtin_amdgcn_permlane16_swap(__float_as_uint(a), __float_as_uint(a), false, false);
  return fmaxf(__uint_as_float(s[0]), __uint_as_float(s[1]));
}
__device__ __forceinline__ float swap32_max(float a) {
  auto s = __builtin_amdgcn_permlane32_swap(__float_as_uint(a), __float_as_uint(a), false, false);
  return fmaxf(__uint_as_float(s[0]), __uint_as_float(s[1]));
}
__device__ __forceinline__ float row_sum(float x) { x += dpp<ROR8>(x); x += dpp<ROR4>(x); x += dpp<ROR2>(x); x += dpp<ROR1>(x); return x; }
__device__ __forceinline__ float wave_sum(float x) { x = row_sum(x); x = swap16_add(x, x); return swap32_add(x, x); }
__device__ __forceinline__ __amdgpu_buffer_rsrc_t mkrsrc(const void* p, unsigned bytes) {
  return __builtin_amdgcn_make_buffer_rsrc(const_cast<void*>(p), 0, bytes, 0x00020000);
}
#define BL128(r, off, aux) __builtin_bit_cast(u32x4, __builtin_amdgcn_raw_buffer_load_b128((r), (off), 0, (aux)))
#define BS128(v, r, off, aux) __builtin_amdgcn_raw_buffer_store_b128((v), (r), (off), 0, (aux))
#define BS64(v, r, off, aux) __builtin_amdgcn_raw_buffer_store_b64((v), (r), (off), 0, (aux))
#define CFENCE() asm volatile("" ::: "memory")
#define SB() __builtin_amdgcn_sched_barrier(0)
#define BARRIER() do { SB(); asm volatile("s_waitcnt lgkmcnt(0)" ::: "memory"); __builtin_amdgcn_s_barrier(); CFENCE(); SB(); } while (0)

#define STAMP(i) __builtin_amdgcn_sched_barrier(0)
#define STAMPV(i) do {} while (0)

__device__ __forceinline__ void layernorm7(float (&v)[7]) {
  const float piv = __builtin_bit_cast(float, __builtin_amdgcn_readfirstlane(__builtin_bit_cast(int, v[0])));
  float s1 = 0.f, s2 = 0.f;
#pragma unroll
  for (int i = 0; i < 7; ++i) { const float d = v[i] - piv; s1 += d; s2 = fmaf(d, d, s2); }
  s1 = wave_sum(s1); s2 = wave_sum(s2);
  const float md = s1 * (1.f / D);
  const float var = fmaxf(s2 * (1.f / D) - md * md, 0.f);
  const float m = rbf(piv + md);
  const float den = rbf(__builtin_amdgcn_sqrtf(rbf(rbf(var) + 1e-5f)));
  const float inv = 1.0f / den;
#pragma unroll
  for (int i = 0; i < 7; ++i) v[i] = rbf(rbf(v[i] - m) * inv);
}

struct Args {
  const u16* x; const u16* kc; const u16* vc; const u16* wq; const u16* wo;
  const u16* w1; const u16* b1; const u16* w2; const u16* b2; u16* out; unsigned char* ws;
};

#define PIN(x) asm volatile("" : "+v"(x))
__device__ __forceinline__ unsigned tagbad(const u32x4& v, unsigned tag) { return (v[1] ^ tag) | (v[3] ^ tag); }
__device__ __forceinline__ void gemv7(const u32x4 (&wr)[7], const float (&xv)[7], float (&y2)[2]) {
  float acc[8];
#pragma unroll
  for (int k = 0; k < 8; ++k) acc[k] = 0.f;
#pragma unroll
  for (int i = 0; i < 7; ++i) {
    const unsigned xb = __float_as_uint(xv[i]), xlo = xb >> 16, xhi = xb;
#pragma unroll
    for (int k = 0; k < 4; ++k) { acc[2 * k] = dot2(wr[i][k], xlo, acc[2 * k]); acc[2 * k + 1] = dot2(wr[i][k], xhi, acc[2 * k + 1]); }
  }
  float y4[4];
#pragma unroll
  for (int k = 0; k < 4; ++k) y4[k] = swap32_add(acc[k], acc[k + 4]);
#pragma unroll
  for (int k = 0; k < 2; ++k) y2[k] = row_sum(swap16_add(y4[k], y4[k + 2]));
}

__global__ void __launch_bounds__(NTHR, 3) fused_decode(Args a) {
  __shared__ __attribute__((aligned(16))) unsigned char smem[2048];
  LAS unsigned* qlds = (LAS unsigned*)smem;
  LAS float* slds = (LAS float*)(smem + 1024);
  const int tid = threadIdx.x, lane = tid & 63, wave = __builtin_amdgcn_readfirstlane(tid >> 6);
  const int b = blockIdx.x, g = b & 7, j = b >> 3;
  STAMP(0);
  asm volatile("" :: "s"(a.x), "s"(a.kc), "s"(a.vc), "s"(a.wq), "s"(a.wo), "s"(a.w1), "s"(a.b1), "s"(a.w2), "s"(a.b2), "s"(a.out), "s"(a.ws));
  const __amdgpu_buffer_rsrc_t rws = mkrsrc(a.ws, WS_END);

  if (wave < 2) {
    const __amdgpu_buffer_rsrc_t rx = mkrsrc(a.x, D * 2);
    unsigned xr[7];
#pragma unroll
    for (int i = 0; i < 7; ++i) xr[i] = __builtin_amdgcn_raw_buffer_load_b16(rx, (lane + 64 * i) * 2, 0, 0);
    u32x4 wr[7];
    const __amdgpu_buffer_rsrc_t rw = mkrsrc(a.wq + (g * 512 + j * 16 + wave * 8), (unsigned)(D * HQ * DH * 2) - (unsigned)((g * 512 + j * 16 + wave * 8) * 2));
#pragma unroll
    for (int i = 0; i < 7; ++i) wr[i] = BL128(rw, (unsigned)((lane + 64 * i) * (HQ * DH * 2)), AUX_W);
    const unsigned tag = __builtin_amdgcn_raw_buffer_load_b32(rws, WS_EPOCH, 0, AUX_SC1) + 1u;
    STAMP(1);
    float xv[7];
#pragma unroll
    for (int i = 0; i < 7; ++i) xv[i] = __uint_as_float(xr[i] << 16);
    layernorm7(xv);
    STAMP(2);
    float y2[2];
    gemv7(wr, xv, y2);
    STAMP(3);
    if ((lane & 15) == 0) { u32x2 gr = {cvtpk(y2[0], y2[1]), tag}; BS64(gr, rws, WS_Q + (unsigned)(g * 2048 + (j * 8 + wave * 4 + (lane >> 4)) * 8), AUX_SC1); }
    STAMP(4);
    if (wave == 0) {
      u32x4 ga, gb;
      unsigned spin = 0;
      for (;; ++spin) {
        CFENCE();
        ga = BL128(rws, WS_Q + (unsigned)(g * 2048 + lane * 16), AUX_SC1);
        gb = BL128(rws, WS_Q + (unsigned)(g * 2048 + 1024 + lane * 16), AUX_SC1);
        const unsigned bad = tagbad(ga, tag) | tagbad(gb, tag);
        if (!__any(bad != 0) || spin > SPIN_CAP) break;
      }
      *(LAS u32x2*)(qlds + 2 * lane) = u32x2{ga[0], ga[2]};
      *(LAS u32x2*)(qlds + 128 + 2 * lane) = u32x2{gb[0], gb[2]};
      STAMP(5);
    }
    BARRIER();
    BARRIER();
  } else if (wave >= 4) {
    const int w = wave - 4;
    const size_t kvoff = ((size_t)g * SK + (SK - WIN) + j * 64) * DH;
    const __amdgpu_buffer_rsrc_t rk = mkrsrc(a.kc + kvoff, 64 * DH * 2), rv = mkrsrc(a.vc + kvoff, 64 * DH * 2);
    u32x4 kr[4], vr[4];
    const unsigned koff = (unsigned)((8 * w + 4 * (lane >> 5)) * 512 + (lane & 31) * 16);
#pragma unroll
    for (int i = 0; i < 4; ++i) kr[i] = BL128(rk, koff + i * 512, AUX_STREAM);
    const unsigned tag = __builtin_amdgcn_raw_buffer_load_b32(rws, WS_EPOCH, 0, AUX_SC1) + 1u;
    const int kq = lane >> 2, dq = lane & 3;
    const unsigned voff = (unsigned)(4 * kq * 512 + w * 64 + dq * 16);
#pragma unroll
    for (int i = 0; i < 4; ++i) vr[i] = BL128(rv, voff + i * 512, AUX_STREAM);
    STAMP(1);
    BARRIER();
    STAMP(2);
    const u32x4 q0 = *(LAS u32x4*)(qlds + 4 * (lane & 31)), q1 = *(LAS u32x4*)(qlds + 128 + 4 * (lane & 31));
    float s[8];
#pragma unroll
    for (int i = 0; i < 4; ++i) {
      float d0 = 0.f, d1 = 0.f;
#pragma unroll
      for (int k = 0; k < 4; ++k) { d0 = dot2(kr[i][k], q0[k], d0); d1 = dot2(kr[i][k], q1[k], d1); }
      s[2 * i] = d0; s[2 * i + 1] = d1;
    }
    f32x4 sv;
#pragma unroll
    for (int k = 0; k < 4; ++k) sv[k] = rbf(row_sum(swap16_add(s[k], s[k + 4]))) * (0.0625f * 1.4426950408889634f);
    if ((lane & 15) == 0) *(LAS f32x4*)(slds + (2 * w + (lane >> 5)) * 8 + ((lane >> 4) & 1) * 4) = sv;
    STAMP(3);
    BARRIER();
    STAMP(4);
    const f32x4 sa = *(LAS f32x4*)(slds + kq * 8), sb = *(LAS f32x4*)(slds + kq * 8 + 4);
    float m0 = fmaxf(fmaxf(sa[0], sa[2]), fmaxf(sb[0], sb[2])), m1 = fmaxf(fmaxf(sa[1], sa[3]), fmaxf(sb[1], sb[3]));
    m0 = fmaxf(m0, dpp<ROR4>(m0)); m1 = fmaxf(m1, dpp<ROR4>(m1));
    m0 = fmaxf(m0, dpp<ROR8>(m0)); m1 = fmaxf(m1, dpp<ROR8>(m1));
    m0 = swap32_max(swap16_max(m0)); m1 = swap32_max(swap16_max(m1));
    const unsigned p01h0 = cvtpk(__builtin_amdgcn_exp2f(sa[0] - m0), __builtin_amdgcn_exp2f(sa[2] - m0));
    const unsigned p23h0 = cvtpk(__builtin_amdgcn_exp2f(sb[0] - m0), __builtin_amdgcn_exp2f(sb[2] - m0));
    const unsigned p01h1 = cvtpk(__builtin_amdgcn_exp2f(sa[1] - m1), __builtin_amdgcn_exp2f(sa[3] - m1));
    const unsigned p23h1 = cvtpk(__builtin_amdgcn_exp2f(sb[1] - m1), __builtin_amdgcn_exp2f(sb[3] - m1));
    STAMP(5);
    STAMPV(9);
    float acc0[8], acc1[8];
#pragma unroll
    for (int k = 0; k < 4; ++k) {
      const unsigned lo01 = __builtin_amdgcn_perm(vr[1][k], vr[0][k], 0x05040100u), hi01 = __builtin_amdgcn_perm(vr[1][k], vr[0][k], 0x07060302u);
      const unsigned lo23 = __builtin_amdgcn_perm(vr[3][k], vr[2][k], 0x05040100u), hi23 = __builtin_amdgcn_perm(vr[3][k], vr[2][k], 0x07060302u);
      acc0[2 * k] = dot2(lo23, p23h0, dot2(lo01, p01h0, 0.f)); acc0[2 * k + 1] = dot2(hi23, p23h0, dot2(hi01, p01h0, 0.f));
      acc1[2 * k] = dot2(lo23, p23h1, dot2(lo01, p01h1, 0.f)); acc1[2 * k + 1] = dot2(hi23, p23h1, dot2(hi01, p01h1, 0.f));
    }
    float x8[8], x4[4];
#pragma unroll
    for (int k = 0; k < 8; ++k) x8[k] = swap32_add(acc0[k], acc1[k]);
#pragma unroll
    for (int k = 0; k < 4; ++k) { float t = swap16_add(x8[k], x8[k + 4]); t += dpp<ROR4>(t); t += dpp<ROR8>(t); x4[k] = t; }
    const int bsel = kq & 1;
    const float v0 = bsel ? x4[2] : x4[0], v1 = bsel ? x4[3] : x4[1];
    const int gi = 4 * dq + 2 * ((lane >> 4) & 1) + bsel, h = lane >> 5;
    const unsigned po = (kq & 2) ? 0x80000000u : WS_PO + (unsigned)((((g * 32 + h * 16 + 2 * w + (gi >> 3)) * 32 + j) * 8 + (gi & 7)) * 16);
    { u32x4 gr = {__float_as_uint(v0), tag, __float_as_uint(v1), tag}; BS128(gr, rws, po, AUX_SC1); }
    if (w == 0) {
      const unsigned ones = 0x3f803f80u;
      float l0 = dot2(p23h0, ones, dot2(p01h0, ones, 0.f)), l1 = dot2(p23h1, ones, dot2(p01h1, ones, 0.f));
      float l = swap32_add(l0, l1); l = swap16_add(l, l); l += dpp<ROR4>(l); l += dpp<ROR8>(l);
      const unsigned mo = (lane & 31) ? 0x80000000u : WS_ML + (unsigned)(((g * 2 + h) * 32 + j) * 16);
      u32x4 gr = {__float_as_uint(h ? m1 : m0), tag, __float_as_uint(l), tag}; BS128(gr, rws, mo, AUX_SC1);
    }
    STAMP(6);
    STAMPV(7);
  } else if (wave == 2) {
    const __amdgpu_buffer_rsrc_t rx = mkrsrc(a.x, D * 2);
    unsigned xr[7];
#pragma unroll
    for (int i = 0; i < 7; ++i) xr[i] = __builtin_amdgcn_raw_buffer_load_b16(rx, (lane + 64 * i) * 2, 0, 0);
    const int u = g * 28 + j;
    const bool mlp = j < 28;
    const __amdgpu_buffer_rsrc_t rb1 = mkrsrc(a.b1 + u * 8, mlp ? 16u : 0u);
    const unsigned bb = __builtin_amdgcn_raw_buffer_load_b32(rb1, (lane >> 4) * 4, 0, 0);
    const unsigned tag = __builtin_amdgcn_raw_buffer_load_b32(rws, WS_EPOCH, 0, AUX_SC1) + 1u;
    float xv[7];
#pragma unroll
    for (int i = 0; i < 7; ++i) xv[i] = __uint_as_float(xr[i] << 16);
    layernorm7(xv);
    layernorm7(xv);
    STAMP(1);
    u32x4 wr[7], wo[16], w2r[8];
    BARRIER();
    const __amdgpu_buffer_rsrc_t rw = mkrsrc(a.w1 + u * 8, mlp ? (unsigned)(D * FF * 2) - (unsigned)(u * 16) : 0u);
#pragma unroll
    for (int i = 0; i < 7; ++i) wr[i] = BL128(rw, (unsigned)((lane + 64 * i) * (FF * 2)), AUX_W);
    const __amdgpu_buffer_rsrc_t r2 = mkrsrc(a.w2 + (size_t)u * 8 * D, mlp ? 8u * D * 2 : 0u);
    const __amdgpu_buffer_rsrc_t ro = mkrsrc(a.wo + (size_t)(g * 512 + j * 16) * D, 16u * D * 2);
    const unsigned co = lane < 56 ? (unsigned)lane * 16u : 0x80000000u;
#pragma unroll
    for (int i = 0; i < 8; ++i) w2r[i] = BL128(r2, co + i * (D * 2), AUX_STREAM);
#pragma unroll
    for (int i = 0; i < 16; ++i) wo[i] = BL128(ro, co + i * (D * 2), AUX_STREAM);
    BARRIER();
    STAMP(2);
    float y2[2];
    gemv7(wr, xv, y2);
    float hh[2];
#pragma unroll
    for (int k = 0; k < 2; ++k) {
      const float bias = __uint_as_float(k ? (bb & 0xffff0000u) : (bb << 16));
      const float z = rbf(rbf(y2[k]) + bias);
      const float inner = 0.7978845608028654f * (z + 0.044715f * z * z * z);
      const float e = __builtin_amdgcn_exp2f(inner * (2.f * 1.4426950408889634f));
      const float th = 1.f - 2.f * __builtin_amdgcn_rcpf(e + 1.f);
      hh[k] = 0.5f * z * (1.f + th);
    }
    const unsigned hp = cvtpk(hh[0], hh[1]);
    float o8[8];
#pragma unroll
    for (int k = 0; k < 8; ++k) o8[k] = 0.f;
#pragma unroll
    for (int r = 0; r < 4; ++r) {
      const unsigned hs = __builtin_amdgcn_readlane(hp, 16 * r);
#pragma unroll
      for (int k = 0; k < 4; ++k) {
        o8[2 * k] = dot2(__builtin_amdgcn_perm(w2r[2 * r + 1][k], w2r[2 * r][k], 0x05040100u), hs, o8[2 * k]);
        o8[2 * k + 1] = dot2(__builtin_amdgcn_perm(w2r[2 * r + 1][k], w2r[2 * r][k], 0x07060302u), hs, o8[2 * k + 1]);
      }
    }
    unsigned wp[8][8];
#pragma unroll
    for (int r = 0; r < 8; ++r)
#pragma unroll
      for (int k = 0; k < 4; ++k) {
        wp[r][2 * k] = __builtin_amdgcn_perm(wo[2 * r + 1][k], wo[2 * r][k], 0x05040100u);
        wp[r][2 * k + 1] = __builtin_amdgcn_perm(wo[2 * r + 1][k], wo[2 * r][k], 0x07060302u);
        PIN(wp[r][2 * k]); PIN(wp[r][2 * k + 1]);
      }
#pragma unroll
    for (int k = 0; k < 8; ++k) PIN(o8[k]);
    STAMP(3);
    const int hsel = j >> 4, sq = lane >> 3;
    u32x4 pd[4], pm[4];
    unsigned spin = 0;
    for (;; ++spin) {
      CFENCE();
#pragma unroll
      for (int i = 0; i < 4; ++i) {
        pd[i] = BL128(rws, WS_PO + (unsigned)((g * 32 + j) * 4096 + i * 1024 + lane * 16), AUX_SC1);
        pm[i] = BL128(rws, WS_ML + (unsigned)(((g * 2 + hsel) * 32 + sq + 8 * i) * 16), AUX_SC1);
      }
      unsigned bad = 0;
#pragma unroll
      for (int i = 0; i < 4; ++i) bad |= tagbad(pd[i], tag) | tagbad(pm[i], tag);
      if (!__any(bad != 0) || spin > SPIN_CAP) break;
    }
    STAMP(4);
    float M = fmaxf(fmaxf(__uint_as_float(pm[0][0]), __uint_as_float(pm[1][0])), fmaxf(__uint_as_float(pm[2][0]), __uint_as_float(pm[3][0])));
    M = fmaxf(M, dpp<ROR8>(M)); M = swap32_max(swap16_max(M));
    float L = 0.f, a0 = 0.f, a1 = 0.f;
#pragma unroll
    for (int i = 0; i < 4; ++i) {
      const float wgt = __builtin_amdgcn_exp2f(__uint_as_float(pm[i][0]) - M);
      L = fmaf(wgt, __uint_as_float(pm[i][2]), L); a0 = fmaf(wgt, __uint_as_float(pd[i][0]), a0); a1 = fmaf(wgt, __uint_as_float(pd[i][2]), a1);
    }
    L += dpp<ROR8>(L); a0 += dpp<ROR8>(a0); a1 += dpp<ROR8>(a1);
    L = swap16_add(L, L); a0 = swap16_add(a0, a0); a1 = swap16_add(a1, a1);
    L = swap32_add(L, L); a0 = swap32_add(a0, a0); a1 = swap32_add(a1, a1);
    const float inv = __builtin_amdgcn_rcpf(L);
    const unsigned op = cvtpk(a0 * inv, a1 * inv);
#pragma unroll
    for (int r = 0; r < 8; ++r) {
      const unsigned os = __builtin_amdgcn_readlane(op, r);
#pragma unroll
      for (int k = 0; k < 8; ++k) o8[k] = dot2(wp[r][k], os, o8[k]);
    }
    const unsigned oo = lane < 56 ? WS_OUT + (unsigned)(lane * 4 * 4096 + b * 16) : 0x80000000u;
#pragma unroll
    for (int k = 0; k < 4; ++k) {
      u32x4 gr = {__float_as_uint(o8[2 * k]), tag, __float_as_uint(o8[2 * k + 1]), tag};
      BS128(gr, rws, oo + k * 4096, AUX_SC1);
    }
    STAMP(5);
    STAMPV(6);
  } else {
    const unsigned tag = __builtin_amdgcn_raw_buffer_load_b32(rws, WS_EPOCH, 0, AUX_SC1) + 1u;
    const bool act = b < 224;
    const __amdgpu_buffer_rsrc_t rx = mkrsrc(a.x, D * 2), rb2 = mkrsrc(a.b2, D * 2);
    const unsigned xx = __builtin_amdgcn_raw_buffer_load_b32(rx, b * 4, 0, 0), bb = __builtin_amdgcn_raw_buffer_load_b32(rb2, b * 4, 0, 0);
    asm volatile("s_waitcnt vmcnt(0)" ::: "memory");
    STAMP(5);
    BARRIER();
    BARRIER();
    STAMP(1);
    if (act) {
      u32x4 pd[4];
      unsigned spin = 0;
      for (;; ++spin) {
        CFENCE();
#pragma unroll
        for (int i = 0; i < 4; ++i) pd[i] = BL128(rws, WS_OUT + (unsigned)(b * 4096 + i * 1024 + lane * 16), AUX_SC1);
        unsigned bad = 0;
#pragma unroll
        for (int i = 0; i < 4; ++i) bad |= tagbad(pd[i], tag);
        if (!__any(bad != 0) || spin > SPIN_CAP) break;
        __builtin_amdgcn_s_sleep(2);
      }
      STAMP(2);
      float s0 = (__uint_as_float(pd[0][0]) + __uint_as_float(pd[1][0])) + (__uint_as_float(pd[2][0]) + __uint_as_float(pd[3][0]));
      float s1 = (__uint_as_float(pd[0][2]) + __uint_as_float(pd[1][2])) + (__uint_as_float(pd[2][2]) + __uint_as_float(pd[3][2]));
      s0 = wave_sum(s0); s1 = wave_sum(s1);
      s0 += __uint_as_float(xx << 16) + __uint_as_float(bb << 16);
      s1 += __uint_as_float(xx & 0xffff0000u) + __uint_as_float(bb & 0xffff0000u);
      if (lane == 0) {
        *(GAS unsigned*)(a.out + 2 * b) = cvtpk(s0, s1);
        if (b == 0) __builtin_amdgcn_raw_buffer_store_b32(tag, rws, WS_EPOCH, 0, AUX_SC1);
      }
      STAMP(3);
      STAMPV(4);
    }
  }
}

extern "C" void kernel_launch(void* const* d_in, const int* in_sizes, int n_in, void* d_out, int out_size, void* d_ws, size_t ws_size, hipStream_t stream) {
  Args a;
  a.x = (const u16*)d_in[0]; a.kc = (const u16*)d_in[1]; a.vc = (const u16*)d_in[2]; a.wq = (const u16*)d_in[3]; a.wo = (const u16*)d_in[4];
  a.w1 = (const u16*)d_in[6]; a.b1 = (const u16*)d_in[7]; a.w2 = (const u16*)d_in[8]; a.b2 = (const u16*)d_in[9];
  a.out = (u16*)d_out; a.ws = (unsigned char*)d_ws;
  fused_decode<<<NWG, NTHR, 0, stream>>>(a);
}
```

```cpp
#include <hip/hip_runtime.h>
#include <hip/hip_bf16.h>
#include <stdint.h>

typedef unsigned short u16;
typedef unsigned u32x4 __attribute__((ext_vector_type(4)));
typedef unsigned u32x2 __attribute__((ext_vector_type(2)));
typedef float f32x4 __attribute__((ext_vector_type(4)));
typedef short s16x4 __attribute__((ext_vector_type(4)));
typedef __bf16 bf16x2 __attribute__((ext_vector_type(2)));
typedef __bf16 bf16x8 __attribute__((ext_vector_type(8)));
#define LAS __attribute__((address_space(3)))
#define GAS __attribute__((address_space(1)))
#define DI __device__ __forceinline__

constexpr int D = 448, SK = 8192, DH = 256, WIN = 2048, FF = 1792;
constexpr int NWG = 256, NWAVE = 7, NTHR = NWAVE * 64;
constexpr unsigned WS_EPOCH = 0;
constexpr unsigned WS_Q = 4096;
constexpr unsigned WS_H = WS_Q + 8 * 8192;
constexpr unsigned WS_S = WS_H + 2 * 14336;
constexpr unsigned WS_OUT = WS_S + 8 * 16384;
constexpr unsigned WS_END = WS_OUT + 256 * 4096;
constexpr unsigned WS_STAMP = 8u << 20;
constexpr unsigned SPIN_CAP = 1u << 18;
constexpr unsigned OOB = 0x80000000u;
constexpr int AUX_SC1 = 16;
constexpr unsigned L_PROJ = 0, L_K = 28672, L_V = 61440, L_WO = 94208, L_MISC = 151552;
constexpr unsigned L_CNT1 = L_MISC, L_CNT2 = L_MISC + 256, L_QF = L_MISC + 512, L_OF = L_MISC + 528, L_RF = L_MISC + 544, L_HF = L_MISC + 560;
constexpr unsigned L_ML = L_MISC + 576;
constexpr unsigned L_ZERO = L_MISC + 768;
constexpr unsigned L_XN = L_MISC + 1024;
constexpr unsigned L_QB = L_XN + 4096;
constexpr unsigned L_PB = L_QB + 1152;
constexpr unsigned L_OB = L_PB + 2560;
constexpr unsigned L_HB = L_OB + 512;
constexpr unsigned L_RED = L_HB + 64;
constexpr unsigned L_XB = L_RED + 128;
constexpr unsigned L_END = L_XB + 2048;
static_assert(L_END <= 163840, "lds");
#define NOSWZ 0
#define SWZ(b) ((NOSWZ & (b)) ? 0u : 1u)
#define EXP 0
#define WIN1 24
#define WIN2 16
#define L2START 32

DI unsigned cvtpk(float lo, float hi) { bf16x2 r; r.x = (__bf16)lo; r.y = (__bf16)hi; return __builtin_bit_cast(unsigned, r); }
DI float rbf(float f) { return __uint_as_float(cvtpk(f, f) & 0xffff0000u); }
DI float blo(unsigned v) { return __uint_as_float(v << 16); }
DI float bhi(unsigned v) { return __uint_as_float(v & 0xffff0000u); }
template <int CTRL> DI float dpp(float x) { return __builtin_bit_cast(float, __builtin_amdgcn_mov_dpp(__builtin_bit_cast(int, x), CTRL, 0xf, 0xf, true)); }
constexpr int ROR1 = 0x121, ROR2 = 0x122, ROR4 = 0x124, ROR8 = 0x128;
DI float swap16_add(float a) { auto s = __builtin_amdgcn_permlane16_swap(__float_as_uint(a), __float_as_uint(a), false, false); return __uint_as_float(s[0]) + __uint_as_float(s[1]); }
DI float swap32_add(float a) { auto s = __builtin_amdgcn_permlane32_swap(__float_as_uint(a), __float_as_uint(a), false, false); return __uint_as_float(s[0]) + __uint_as_float(s[1]); }
DI float swap16_max(float a) { auto s = __builtin_amdgcn_permlane16_swap(__float_as_uint(a), __float_as_uint(a), false, false); return fmaxf(__uint_as_float(s[0]), __uint_as_float(s[1])); }
DI float swap32_max(float a) { auto s = __builtin_amdgcn_permlane32_swap(__float_as_uint(a), __float_as_uint(a), false, false); return fmaxf(__uint_as_float(s[0]), __uint_as_float(s[1])); }
DI float wave_sum(float x) { x += dpp<ROR8>(x); x += dpp<ROR4>(x); x += dpp<ROR2>(x); x += dpp<ROR1>(x); return swap32_add(swap16_add(x)); }
DI __amdgpu_buffer_rsrc_t mkrsrc(const void* p, unsigned bytes) { return __builtin_amdgcn_make_buffer_rsrc(const_cast<void*>(p), 0, bytes, 0x00020000); }
#define BL128(r, off, aux) __builtin_bit_cast(u32x4, __builtin_amdgcn_raw_buffer_load_b128((r), (off), 0, (aux)))
#define BS128(v, r, off, aux) __builtin_amdgcn_raw_buffer_store_b128((v), (r), (off), 0, (aux))
#define CFENCE() asm volatile("" ::: "memory")
#define SB() __builtin_amdgcn_sched_barrier(0)
DI unsigned rfl(unsigned v) { return __builtin_amdgcn_readfirstlane(v); }
DI unsigned ld32(unsigned a) { return *(LAS unsigned*)(size_t)a; }
DI u32x2 ld64(unsigned a) { return *(LAS u32x2*)(size_t)a; }
DI u32x4 ld128(unsigned a) { return *(LAS u32x4*)(size_t)a; }
DI void st32(unsigned a, unsigned v) { *(LAS unsigned*)(size_t)a = v; }
DI void st64(unsigned a, u32x2 v) { *(LAS u32x2*)(size_t)a = v; }
DI void st128(unsigned a, u32x4 v) { *(LAS u32x4*)(size_t)a = v; }
DI u32x2 ldtr(unsigned a) { return __builtin_bit_cast(u32x2, __builtin_amdgcn_ds_read_tr16_b64_v4i16((LAS s16x4*)(size_t)a)); }
DI u32x4 ldtr2(unsigned a, unsigned b) { u32x2 x = ldtr(a), y = ldtr(b); return u32x4{x[0], x[1], y[0], y[1]}; }
DI f32x4 mfma(u32x4 a, u32x4 b, f32x4 c) { return __builtin_amdgcn_mfma_f32_16x16x32_bf16(__builtin_bit_cast(bf16x8, a), __builtin_bit_cast(bf16x8, b), c, 0, 0, 0); }
DI unsigned tagbad(const u32x4& v, unsigned tag) { return (v[1] ^ tag) | (v[3] ^ tag); }

#define STAMP(i) do {} while (0)
#define SPINS(n) do {} while (0)

DI void layernorm8(float (&v)[8], bool valid) {
  const float piv = __builtin_bit_cast(float, __builtin_amdgcn_readfirstlane(__builtin_bit_cast(int, v[0])));
  float s1 = 0.f, s2 = 0.f;
#pragma unroll
  for (int i = 0; i < 8; ++i) { const float d = v[i] - piv; s1 += d; s2 = fmaf(d, d, s2); }
  s1 = valid ? s1 : 0.f; s2 = valid ? s2 : 0.f;
  s1 = wave_sum(s1); s2 = wave_sum(s2);
  const float md = s1 * (1.f / D);
  const float var = fmaxf(s2 * (1.f / D) - md * md, 0.f);
  const float m = rbf(piv + md);
  const float den = rbf(__builtin_amdgcn_sqrtf(rbf(rbf(var) + 1e-5f)));
  const float inv = 1.0f / den;
#pragma unroll
  for (int i = 0; i < 8; ++i) v[i] = rbf(rbf(v[i] - m) * inv);
}

struct Args {
  const u16* x; const u16* kc; const u16* vc; const u16* wq; const u16* wo;
  const u16* w1; const u16* b1; const u16* w2; const u16* b2; u16* out; unsigned char* ws;
};

#define DMA4(NT, lds, r, soff, v0, v1, v2, v3) asm volatile("s_mov_b32 m0, %0\n\ts_nop 0\n\t" \
    "buffer_load_dwordx4 %3, %1, %2 offen " NT " lds\n\tbuffer_load_dwordx4 %4, %1, %2 offen offset:1024 " NT " lds\n\t" \
    "buffer_load_dwordx4 %5, %1, %2 offen offset:2048 " NT " lds\n\tbuffer_load_dwordx4 %6, %1, %2 offen offset:3072 " NT " lds" \
    :: "s"(lds), "s"(r), "s"(soff), "v"(v0), "v"(v1), "v"(v2), "v"(v3) : "memory")
#define LANDED(N, addr, val) asm volatile("s_waitcnt vmcnt(%c0)\n\tds_write_b32 %1, %2" :: "i"(N), "v"(addr), "v"(val) : "memory")

template <int K> DI void l1_after(unsigned ca) {
  if constexpr (4 * (K + 1) > WIN1 - 4) { LANDED(WIN1 - 4, ca, (unsigned)(4 * (K + 1) - (WIN1 - 4))); }
}
template <int R> DI void l1_tail(unsigned ca) {
  if constexpr (R >= 0) { LANDED(R, ca, (unsigned)(92 - R)); l1_tail<R - 2>(ca); }
}

__global__ void __launch_bounds__(NTHR, 2) fused_decode(Args a) {
  __shared__ __attribute__((aligned(16))) unsigned char smem[163840];
  const unsigned lane = threadIdx.x & 63, wave = rfl(threadIdx.x >> 6);
  const unsigned b = blockIdx.x, g = b & 7, j = b >> 3, c = j & 7, s = j >> 3;
  STAMP(0);
  asm volatile("" :: "s"((unsigned)(size_t)(LAS unsigned char*)smem));

  if (wave == 0) {
    __builtin_amdgcn_s_setprio(3);
    const unsigned tq = j < 16, th = j < 23;
    const unsigned T = g * 7 + j - 16, U = T - 7;
    const unsigned S = tq ? 8192u : th ? 3584u : 128u;
    const u16* pb = tq ? a.wq : th ? a.w1 : a.w2;
    const unsigned pbytes = tq ? 448u * 8192u : j < 30 ? 448u * 3584u : 0u;
    unsigned so = tq ? (j & 1) * 224u * 8192u + (g * 8 + (j >> 1)) * 128u : th ? (T & 1) * 224u * 3584u + (T >> 1) * 128u : U * 28672u;
    const __amdgpu_buffer_rsrc_t rp = mkrsrc(pb, pbytes);
    const unsigned p0 = (lane >> 3) * S + (((lane & 7) << 4) ^ (SWZ(1) * ((lane >> 4) & 3) << 5)), ps = 8 * S - 1024;
    const unsigned p1 = p0 + ps, p2 = p1 + ps, p3 = p2 + ps, gs = 32 * S;
    const unsigned ca = L_CNT1 + lane * 4;
    unsigned cnt = 0;
    asm volatile("" :: "s"(rp), "v"(p3));
    STAMP(5);
    asm volatile("ds_write_b32 %[ca], %[cnt]\n\ts_mov_b32 m0, 0\n"
      "1:\n\ts_nop 0\n\t"
      "buffer_load_dwordx4 %[p0], %[r], %[so] offen lds\n\tbuffer_load_dwordx4 %[p1], %[r], %[so] offen offset:1024 lds\n\t"
      "buffer_load_dwordx4 %[p2], %[r], %[so] offen offset:2048 lds\n\tbuffer_load_dwordx4 %[p3], %[r], %[so] offen offset:3072 lds\n\t"
      "s_add_u32 %[so], %[so], %[gs]\n\ts_add_u32 m0, m0, 0x1000\n\ts_cmp_lg_u32 m0, %[e]\n\ts_cbranch_scc1 1b\n\ts_waitcnt lgkmcnt(0)"
      : [so] "+s"(so) : [r] "s"(rp), [gs] "s"(gs), [p0] "v"(p0), [p1] "v"(p1), [p2] "v"(p2), [p3] "v"(p3), [ca] "v"(ca), [cnt] "v"(cnt), [e] "i"((WIN1 - 4) * 1024) : "memory", "scc");
    STAMP(1);
    __builtin_amdgcn_s_barrier();
    const size_t kvo = ((size_t)g * SK + (SK - WIN)) * DH;
    const __amdgpu_buffer_rsrc_t rk = mkrsrc(a.kc + kvo + j * (64 * DH), 32768u), rv = mkrsrc(a.vc + kvo + s * (512 * DH) + c * 32, 512u * 512u - 448u);
    unsigned kv[8];
#pragma unroll
    for (int i = 0; i < 8; ++i) kv[i] = (lane >> 5) * 512 + (((lane & 31) ^ (SWZ(2) * (2 * i + (lane >> 5)) & 15)) << 4);
    const unsigned v0 = (lane >> 2) * 512 + (((lane & 3) ^ (SWZ(4) * (lane >> 3) & 2)) << 4), v1 = v0 + 7168, v2 = v0 + 14336, v3 = v0 + 21504;
#define G4(NT, r, so, a0, a1, a2, a3) "s_nop 0\n\tbuffer_load_dwordx4 %[" a0 "], %[" r "], %[" so "] offen " NT " lds\n\tbuffer_load_dwordx4 %[" a1 "], %[" r "], %[" so "] offen offset:1024 " NT " lds\n\t" \
      "buffer_load_dwordx4 %[" a2 "], %[" r "], %[" so "] offen offset:2048 " NT " lds\n\tbuffer_load_dwordx4 %[" a3 "], %[" r "], %[" so "] offen offset:3072 " NT " lds\n\t" \
      "v_add_u32 %[cnt], 4, %[cnt]\n\ts_add_u32 m0, m0, 0x1000\n\ts_waitcnt vmcnt(%c[wn])\n\tds_write_b32 %[ca], %[cnt]\n\t"
    unsigned sk = 0, sv = 0;
    STAMP(2);
    asm volatile("s_mov_b32 m0, %[m]\n"
      "1:\n\t" G4("", "rp", "so", "p0", "p1", "p2", "p3") "s_add_u32 %[so], %[so], %[gs]\n\ts_cmp_lg_u32 m0, %[e0]\n\ts_cbranch_scc1 1b\n"
      "2:\n\t" G4("nt", "rk", "sk", "k0", "k1", "k2", "k3") "s_add_u32 %[sk], %[sk], 0x1000\n\t" G4("nt", "rk", "sk", "k4", "k5", "k6", "k7") "s_add_u32 %[sk], %[sk], 0x1000\n\ts_cmp_lg_u32 m0, %[e1]\n\ts_cbranch_scc1 2b\n"
      "3:\n\t" G4("nt", "rv", "sv", "v0", "v1", "v2", "v3") "s_add_u32 %[sv], %[sv], 0x8000\n\ts_cmp_lg_u32 m0, %[e2]\n\ts_cbranch_scc1 3b"
      : [so] "+s"(so), [sk] "+s"(sk), [sv] "+s"(sv), [cnt] "+v"(cnt)
      : [rp] "s"(rp), [rk] "s"(rk), [rv] "s"(rv), [gs] "s"(gs), [p0] "v"(p0), [p1] "v"(p1), [p2] "v"(p2), [p3] "v"(p3),
        [k0] "v"(kv[0]), [k1] "v"(kv[1]), [k2] "v"(kv[2]), [k3] "v"(kv[3]), [k4] "v"(kv[4]), [k5] "v"(kv[5]), [k6] "v"(kv[6]), [k7] "v"(kv[7]),
        [v0] "v"(v0), [v1] "v"(v1), [v2] "v"(v2), [v3] "v"(v3), [ca] "v"(ca), [m] "i"((WIN1 - 4) * 1024), [e0] "i"(L_K), [e1] "i"(L_V), [e2] "i"(L_WO), [wn] "i"(WIN1 - 4) : "memory", "scc");
    STAMP(3);
    l1_tail<WIN1 - 6>(ca);
    STAMP(4);
  } else if (wave == 1) {
    __builtin_amdgcn_s_setprio(2);
    const __amdgpu_buffer_rsrc_t ro = mkrsrc(a.wo + (size_t)(g * 512 + c * 32) * D, (256u + 32u) * 896u);
    const unsigned vo = (lane << 4) ^ (SWZ(8) * ((lane >> 4) & 3) << 5);
    __builtin_amdgcn_s_barrier();
    if (!(EXP & 32)) {
_Pragma("nounroll")
    for (unsigned spin = 0; spin < SPIN_CAP; ++spin) { CFENCE(); if (rfl(ld32(L_CNT1)) >= L2START) break; __builtin_amdgcn_s_sleep(2); }
    STAMP(1);
    unsigned gi = (s * 7 + 1) >> 1;
#pragma unroll 1
    for (int k = 0; k < 14; ++k) {
      const unsigned so = gi * 4096u + (gi >= 7 ? 256u * 896u - 28672u : 0u);
      DMA4("", L_WO + 4096u * gi, ro, so, vo, vo, vo, vo);
      if (k == 3) { asm volatile("s_waitcnt vmcnt(0)" ::: "memory"); STAMP(2); } else asm volatile("s_waitcnt vmcnt(%c0)" :: "i"(WIN2 - 4) : "memory");
      gi = gi == 13 ? 0u : gi + 1;
    }
    STAMP(3);
    LANDED(0, L_CNT2 + lane * 4, 1u);
    STAMP(4);
    }
  } else if (wave == 6) {
    __builtin_amdgcn_s_barrier();
    if (j >= 23 && j < 30 && !(EXP & 8)) {
      const unsigned U = g * 7 + j - 23;
      const __amdgpu_buffer_rsrc_t rws = mkrsrc(a.ws, WS_END), rb1 = mkrsrc(a.b1, FF * 2);
      const unsigned tag = (EXP & 4) ? 0x12345678u : *(const __attribute__((address_space(4))) unsigned*)(a.ws + WS_EPOCH) + 1u;
      const unsigned bb = __builtin_amdgcn_raw_buffer_load_b32(rb1, U * 64 + (lane & 15) * 4, 0, 0);
_Pragma("nounroll")
      for (unsigned spin = 0; spin < SPIN_CAP; ++spin) { CFENCE(); if (rfl(ld32(L_CNT1)) >= 28) break; __builtin_amdgcn_s_sleep(4); }
      STAMP(1);
      const unsigned ho = WS_H + ((lane >> 4) & 1) * 14336u + (U * 16 + (lane & 15)) * 16;
      u32x4 hv; unsigned spin = 0;
_Pragma("nounroll")
      for (;; ++spin) {
        CFENCE();
        hv = BL128(rws, ho, AUX_SC1);
        if (!__any(tagbad(hv, tag) != 0) || spin > SPIN_CAP) break;
        __builtin_amdgcn_s_sleep(4);
      }
      SPINS(spin);
      STAMP(2);
      float hh[2];
#pragma unroll
      for (int k = 0; k < 2; ++k) {
        const float z = rbf(rbf(swap16_add(__uint_as_float(hv[2 * k]))) + (k ? bhi(bb) : blo(bb)));
        const float inner = 0.7978845608028654f * (z + 0.044715f * z * z * z);
        const float e = __builtin_amdgcn_exp2f(inner * (2.f * 1.4426950408889634f));
        const float th = 1.f - 2.f * __builtin_amdgcn_rcpf(e + 1.f);
        hh[k] = 0.5f * z * (1.f + th);
      }
      const unsigned e = lane & 15;
      st32(L_HB + (4 * ((e >> 1) & 3) + 2 * (e >> 3) + (e & 1)) * 4, cvtpk(hh[0], hh[1]));
      CFENCE();
      const unsigned grp = lane >> 4, n = lane & 15;
      u32x4 av[4];
#pragma unroll
      for (int v = 0; v < 4; ++v) av[v] = ld128(n == 4u * v ? L_HB + grp * 16 : L_ZERO);
      const unsigned o0 = (4 * grp + (n >> 2)) * 896 + (lane & 3) * 8;
#pragma unroll 1
      for (unsigned w = 0; w < 4; ++w) {
        f32x4 x0 = {0.f, 0.f, 0.f, 0.f}, x1 = x0;
#pragma unroll
        for (int t = 0; t < 7; ++t) {
          unsigned o = o0 + w * 32 + t * 128; o ^= ((o >> 8) & 3) << 5;
          const u32x4 bf = ldtr2(L_PROJ + o, L_PROJ + o + 14336);
          if (t & 1) x1 = mfma(av[t >> 1], bf, x1); else x0 = mfma(av[t >> 1], bf, x0);
        }
        st64(L_XB + w * 512 + lane * 8, u32x2{__float_as_uint(x0[0]), __float_as_uint(x1[0])});
      }
      CFENCE();
      st32(L_HF, 1u);
      STAMP(3);
    }
  } else {
    const unsigned w = wave - 2, grp = lane >> 4, n = lane & 15, q = n >> 2, p = lane & 3;
    const __amdgpu_buffer_rsrc_t rx = mkrsrc(a.x, D * 2), rws = mkrsrc(a.ws, WS_END);
    u32x4 xr = BL128(rx, (EXP & 2) && w ? OOB : lane * 16, 0);
    if (w == 0 && lane >= 16) st128(L_MISC + lane * 16, u32x4{0u, 0u, 0u, 0u});
    asm volatile("s_waitcnt lgkmcnt(0)" ::: "memory");
    __builtin_amdgcn_s_barrier();
    CFENCE();
    SB();
    const unsigned tag = (EXP & 4) ? 0x12345678u : *(const volatile __attribute__((address_space(4))) unsigned*)(a.ws + WS_EPOCH) + 1u;
    SB();
    asm volatile("" : "+v"(xr));
    if (EXP & 64) __builtin_amdgcn_endpgm();
    const unsigned krow = 16 * (grp >> 1) + 4 * (grp & 1) + q;
    if (j < 23) {
      const unsigned T = g * 7 + j - 16;
      const unsigned half = j < 16 ? (j & 1) : (T & 1);
      float xv[8];
#pragma unroll
      for (int i = 0; i < 4; ++i) { xv[2 * i] = blo(xr[i]); xv[2 * i + 1] = bhi(xr[i]); }
#pragma unroll 1
      for (unsigned r = j < 16 ? 1u : 0u; r < 2; ++r) layernorm8(xv, lane < 56);
      const unsigned xa = L_XN + w * 1024 + (lane >> 2) * 64 + (4 * ((lane >> 1) & 1) + (lane & 1)) * 8;
      st64(xa, u32x2{__builtin_amdgcn_perm(__float_as_uint(xv[1]), __float_as_uint(xv[0]), 0x07060302u), __builtin_amdgcn_perm(__float_as_uint(xv[3]), __float_as_uint(xv[2]), 0x07060302u)});
      st64(xa + 16, u32x2{__builtin_amdgcn_perm(__float_as_uint(xv[5]), __float_as_uint(xv[4]), 0x07060302u), __builtin_amdgcn_perm(__float_as_uint(xv[7]), __float_as_uint(xv[6]), 0x07060302u)});
      CFENCE();
      u32x4 xf[7];
#pragma unroll
      for (int ks = 0; ks < 7; ++ks) xf[ks] = ld128(L_XN + w * 1024 + half * 448 + ks * 64 + grp * 16);
      STAMP(1);
      const unsigned pa = L_PROJ + krow * 128 + ((w ^ ((krow >> 1) & 3)) << 5) + p * 8;
_Pragma("nounroll")
      for (unsigned spin = 0; spin < SPIN_CAP; ++spin) { CFENCE(); if (rfl(ld32(L_CNT1)) >= 24) break; if (EXP & 16) __builtin_amdgcn_s_sleep(4); }
      CFENCE();
      f32x4 acc = {0.f, 0.f, 0.f, 0.f};
#pragma unroll
      for (int ks = 0; ks < 6; ++ks) acc = mfma(ldtr2(pa + ks * 4096, pa + ks * 4096 + 1024), xf[ks], acc);
      u32x4 wl;
_Pragma("nounroll")
      for (unsigned spin = 0;; ++spin) {
        CFENCE(); const unsigned cn = ld32(L_CNT1); CFENCE();
        wl = ldtr2(pa + 6 * 4096, pa + 6 * 4096 + 1024);
        if (rfl(cn) >= 28 || spin > SPIN_CAP) break;
      }
      STAMP(2);
      acc = mfma(wl, xf[6], acc);
      const unsigned pbase = j < 16 ? WS_Q + g * 8192 + half * 4096 + (j >> 1) * 512 : WS_H + half * 14336 + (T >> 1) * 512;
      const u32x4 gr = {__float_as_uint(n ? acc[2] : acc[0]), tag, __float_as_uint(n ? acc[3] : acc[1]), tag};
      BS128(gr, rws, n < 2 ? pbase + (8 * w + 2 * grp + n) * 16 : OOB, AUX_SC1);
      STAMP(3);
    }
    if (EXP & 1) { _Pragma("nounroll") for (unsigned spin = 0; spin < SPIN_CAP; ++spin) { CFENCE(); if (rfl(ld32(L_CNT1)) >= 32) break; __builtin_amdgcn_s_sleep(2); } }
    if (EXP & 8) __builtin_amdgcn_endpgm();
    const unsigned ka = L_K + (16 * w + n) * 512;
    u32x4 kf[8];
    unsigned kok = 0;
    {
      const unsigned qo = WS_Q + g * 8192 + w * 1024 + lane * 16;
      u32x4 qa, qb; unsigned spin = 0;
_Pragma("nounroll")
      for (;; ++spin) {
        CFENCE();
        qa = BL128(rws, qo, AUX_SC1); qb = BL128(rws, qo + 4096, AUX_SC1);
        if (!kok && rfl(ld32(L_CNT1)) >= 36 + 8 * w) {
          CFENCE();
#pragma unroll
          for (int ks = 0; ks < 8; ++ks) kf[ks] = ld128(ka + (((4 * ks + grp) ^ n) << 4));
          kok = 1;
        }
        if (!__any((tagbad(qa, tag) | tagbad(qb, tag)) != 0) || spin > SPIN_CAP) break;
      }
      SPINS(spin);
      st32(L_QB + (w >> 1) * 576 + (w & 1) * 256 + lane * 4, cvtpk(__uint_as_float(qa[0]) + __uint_as_float(qb[0]), __uint_as_float(qa[2]) + __uint_as_float(qb[2])));
      CFENCE();
      st32(L_QF + w * 4, 1u);
      STAMP(4);
    }
    if (!kok) {
_Pragma("nounroll")
      for (unsigned spin = 0;; ++spin) {
        CFENCE(); const unsigned cn = ld32(L_CNT1); CFENCE();
#pragma unroll
        for (int ks = 0; ks < 8; ++ks) kf[ks] = ld128(ka + (((4 * ks + grp) ^ n) << 4));
        if (rfl(cn) >= 36 + 8 * w || spin > SPIN_CAP) break;
      }
    }
    {
      u32x4 qf[8];
      const unsigned qa = L_QB + (n & 1) * 576 + grp * 16;
_Pragma("nounroll")
      for (unsigned spin = 0;; ++spin) {
        CFENCE(); const u32x4 fl = ld128(L_QF); CFENCE();
#pragma unroll
        for (int ks = 0; ks < 8; ++ks) qf[ks] = ld128(qa + ks * 64);
        if (rfl(fl[0] & fl[1] & fl[2] & fl[3]) || spin > SPIN_CAP) break;
      }
      STAMP(5);
      f32x4 s0 = {0.f, 0.f, 0.f, 0.f}, s1 = s0;
#pragma unroll
      for (int ks = 0; ks < 4; ++ks) { s0 = mfma(kf[2 * ks], qf[2 * ks], s0); s1 = mfma(kf[2 * ks + 1], qf[2 * ks + 1], s1); }
      s0 += s1;
      const u32x4 gr = {cvtpk(s0[0], s0[1]), tag, cvtpk(s0[2], s0[3]), tag};
      BS128(gr, rws, n < 2 ? WS_S + g * 16384 + j * 512 + w * 128 + (grp * 2 + n) * 16 : OOB, AUX_SC1);
      STAMP(6);
    }
    float mw;
    {
      u32x4 sv[4]; unsigned spin = 0;
      const unsigned so = WS_S + g * 16384 + w * 1024 + lane * 16;
_Pragma("nounroll")
      for (;; ++spin) {
        CFENCE();
        unsigned bad = 0;
#pragma unroll
        for (int i = 0; i < 4; ++i) sv[i] = BL128(rws, so + ((s + i) & 3) * 4096, AUX_SC1);
#pragma unroll
        for (int i = 0; i < 4; ++i) bad |= tagbad(sv[i], tag);
        if (!__any(bad != 0) || spin > SPIN_CAP) break;
      }
      SPINS(spin);
      STAMP(7);
      float e[16];
#pragma unroll
      for (int i = 0; i < 4; ++i) { e[4 * i] = blo(sv[i][0]); e[4 * i + 1] = bhi(sv[i][0]); e[4 * i + 2] = blo(sv[i][2]); e[4 * i + 3] = bhi(sv[i][2]); }
      float m = e[0];
#pragma unroll
      for (int i = 1; i < 16; ++i) m = fmaxf(m, e[i]);
      m = fmaxf(m, dpp<ROR2>(m)); m = fmaxf(m, dpp<ROR4>(m)); m = fmaxf(m, dpp<ROR8>(m)); m = swap32_max(swap16_max(m));
      constexpr float CS = 0.0625f * 1.4426950408889634f;
      mw = m * CS;
#pragma unroll
      for (int i = 0; i < 4; ++i) e[i] = __builtin_amdgcn_exp2f(fmaf(e[i], CS, -mw));
      const unsigned Q = lane >> 1;
      st64(L_PB + w * 640 + (lane & 1) * 320 + (Q & ~3u) * 8 + (Q & 1) * 16 + (Q & 2) * 4, u32x2{cvtpk(e[0], e[1]), cvtpk(e[2], e[3])});
      float l = 0.f;
#pragma unroll
      for (int i = 4; i < 16; ++i) e[i] = __builtin_amdgcn_exp2f(fmaf(e[i], CS, -mw));
#pragma unroll
      for (int i = 0; i < 16; ++i) l += e[i];
      l += dpp<ROR2>(l); l += dpp<ROR4>(l); l += dpp<ROR8>(l); l = swap32_add(swap16_add(l));
      if (lane < 2) st64(L_ML + lane * 32 + w * 8, u32x2{__float_as_uint(mw), __float_as_uint(l)});
      CFENCE();
    }
    u32x4 pf[4];
#pragma unroll
    for (int ks = 0; ks < 4; ++ks) pf[ks] = ld128(L_PB + w * 640 + (n & 1) * 320 + ks * 64 + grp * 16);
    STAMP(8);
    u32x4 of[14];
_Pragma("nounroll")
    for (unsigned spin = 0; spin < SPIN_CAP; ++spin) { CFENCE(); if (rfl(ld32(L_CNT2))) break; }
    CFENCE();
    {
      const unsigned o0 = (4 * grp + q) * 896 + w * 32 + p * 8;
#pragma unroll
      for (int t = 0; t < 7; ++t) {
        unsigned o = o0 + t * 128; o ^= ((o >> 8) & 3) << 5; o += L_WO;
        of[2 * t] = ldtr2(o, o + 14336); of[2 * t + 1] = ldtr2(o + 28672, o + 28672 + 14336);
      }
    }
    STAMP(9);
    {
      const unsigned va0 = L_V + w * 8192 + krow * 64 + ((grp & 1) << 5) + p * 8, va1 = va0 ^ 32;
      f32x4 o0 = {0.f, 0.f, 0.f, 0.f}, o1 = o0;
_Pragma("nounroll")
      for (unsigned spin = 0; spin < SPIN_CAP; ++spin) { CFENCE(); if (rfl(ld32(L_CNT1)) >= 66 + 8 * w) break; }
      CFENCE();
#pragma unroll
      for (int ks = 0; ks < 3; ++ks) {
        o0 = mfma(ldtr2(va0 + ks * 2048, va0 + ks * 2048 + 512), pf[ks], o0);
        o1 = mfma(ldtr2(va1 + ks * 2048, va1 + ks * 2048 + 512), pf[ks], o1);
      }
      u32x4 ma, mb;
_Pragma("nounroll")
      for (unsigned spin = 0;; ++spin) {
        CFENCE();
        ma = ld128(L_ML + (n & 1) * 32); mb = ld128(L_ML + (n & 1) * 32 + 16);
        if (!__any(ma[1] == 0 || ma[3] == 0 || mb[1] == 0 || mb[3] == 0) || spin > SPIN_CAP) break;
      }
      const float M = fmaxf(fmaxf(__uint_as_float(ma[0]), __uint_as_float(ma[2])), fmaxf(__uint_as_float(mb[0]), __uint_as_float(mb[2])));
      const float L = (__uint_as_float(ma[1]) * __builtin_amdgcn_exp2f(__uint_as_float(ma[0]) - M) + __uint_as_float(ma[3]) * __builtin_amdgcn_exp2f(__uint_as_float(ma[2]) - M))
                    + (__uint_as_float(mb[1]) * __builtin_amdgcn_exp2f(__uint_as_float(mb[0]) - M) + __uint_as_float(mb[3]) * __builtin_amdgcn_exp2f(__uint_as_float(mb[2]) - M));
      const float f = __builtin_amdgcn_exp2f(mw - M) * __builtin_amdgcn_rcpf(L);
      u32x4 va, vb;
_Pragma("nounroll")
      for (unsigned spin = 0;; ++spin) {
        CFENCE(); const unsigned cn = ld32(L_CNT1); CFENCE();
        va = ldtr2(va0 + 3 * 2048, va0 + 3 * 2048 + 512); vb = ldtr2(va1 + 3 * 2048, va1 + 3 * 2048 + 512);
        if (rfl(cn) >= 68 + 8 * w || spin > SPIN_CAP) break;
      }
      STAMP(10);
      o0 = mfma(va, pf[3], o0); o1 = mfma(vb, pf[3], o1);
      if (n < 2) st128(L_OB + (w * 2 + n) * 64 + grp * 16, u32x4{cvtpk(o0[0] * f, o0[1] * f), cvtpk(o0[2] * f, o0[3] * f), cvtpk(o1[0] * f, o1[1] * f), cvtpk(o1[2] * f, o1[3] * f)});
      CFENCE();
      st32(L_OF + w * 4, 1u);
    }
    {
      const bool hw = j >= 23 && j < 30;
      u32x4 av[8]; u32x2 xb;
      const unsigned oa = L_OB + (n & 3) * 128 + grp * 16;
_Pragma("nounroll")
      for (unsigned spin = 0;; ++spin) {
        CFENCE(); const u32x4 fl = ld128(L_OF); const unsigned hf = ld32(L_HF); CFENCE();
#pragma unroll
        for (int v = 0; v < 4; ++v) { const unsigned aa = q == (unsigned)v ? oa : L_ZERO; av[2 * v] = ld128(aa); av[2 * v + 1] = ld128(aa + 64); }
        xb = ld64(hw ? L_XB + w * 512 + lane * 8 : L_ZERO);
        if (rfl(fl[0] & fl[1] & fl[2] & fl[3] & (hw ? hf : 1u)) || spin > SPIN_CAP) break;
      }
      STAMP(11);
      f32x4 x0 = {0.f, 0.f, 0.f, 0.f}, x1 = x0;
      x0[0] = __uint_as_float(xb[0]); x1[0] = __uint_as_float(xb[1]);
#pragma unroll
      for (int t = 0; t < 7; ++t) {
        if (t & 1) { x1 = mfma(av[t & ~1], of[2 * t], x1); x1 = mfma(av[(t & ~1) + 1], of[2 * t + 1], x1); }
        else { x0 = mfma(av[t], of[2 * t], x0); x0 = mfma(av[t + 1], of[2 * t + 1], x0); }
      }
      const u32x4 gr = {__float_as_uint((x0[0] + x0[1]) + (x0[2] + x0[3])), tag, __float_as_uint((x1[0] + x1[1]) + (x1[2] + x1[3])), tag};
      BS128(gr, rws, WS_OUT + b * 4096 + w * 1024 + lane * 16, AUX_SC1);
    }
    if (j >= 24) {
      const unsigned cid = g * 8 + j - 24;
      const unsigned col = 128 * ((cid >> 2) & 3) + 16 * (cid >> 4) + 4 * (cid & 3) + p;
      const __amdgpu_buffer_rsrc_t rb2 = mkrsrc(a.b2, D * 2);
      const unsigned x0 = __builtin_amdgcn_raw_buffer_load_b16(rx, col * 2, 0, 0), x1 = __builtin_amdgcn_raw_buffer_load_b16(rx, col * 2 + 128, 0, 0);
      const unsigned c0 = __builtin_amdgcn_raw_buffer_load_b16(rb2, col * 2, 0, 0), c1 = __builtin_amdgcn_raw_buffer_load_b16(rb2, col * 2 + 128, 0, 0);
      const unsigned oo = WS_OUT + (w * 64 + (lane >> 2)) * 4096 + cid * 64 + p * 16;
      u32x4 pd[4]; unsigned spin = 0;
_Pragma("nounroll")
      for (;; ++spin) {
        CFENCE();
        unsigned bad = 0;
#pragma unroll
        for (int i = 0; i < 4; ++i) pd[i] = BL128(rws, oo + i * 65536, AUX_SC1);
#pragma unroll
        for (int i = 0; i < 4; ++i) bad |= tagbad(pd[i], tag);
        if (!__any(bad != 0) || spin > SPIN_CAP) break;
      }
      SPINS(spin);
      float r0 = (__uint_as_float(pd[0][0]) + __uint_as_float(pd[1][0])) + (__uint_as_float(pd[2][0]) + __uint_as_float(pd[3][0]));
      float r1 = (__uint_as_float(pd[0][2]) + __uint_as_float(pd[1][2])) + (__uint_as_float(pd[2][2]) + __uint_as_float(pd[3][2]));
      r0 += dpp<ROR4>(r0); r1 += dpp<ROR4>(r1); r0 += dpp<ROR8>(r0); r1 += dpp<ROR8>(r1);
      r0 = swap32_add(swap16_add(r0)); r1 = swap32_add(swap16_add(r1));
      if (w != 0) {
        if (lane < 4) st64(L_RED + w * 32 + lane * 8, u32x2{__float_as_uint(r0), __float_as_uint(r1)});
        CFENCE();
        st32(L_RF + w * 4, 1u);
      } else {
        u32x2 t1, t2, t3;
_Pragma("nounroll")
        for (unsigned sp = 0;; ++sp) {
          CFENCE(); const u32x4 fl = ld128(L_RF); CFENCE();
          t1 = ld64(L_RED + 32 + p * 8); t2 = ld64(L_RED + 64 + p * 8); t3 = ld64(L_RED + 96 + p * 8);
          if (rfl(fl[1] & fl[2] & fl[3]) || sp > SPIN_CAP) break;
        }
        r0 = (r0 + __uint_as_float(t1[0])) + (__uint_as_float(t2[0]) + __uint_as_float(t3[0])) + (blo(x0) + blo(c0));
        r1 = (r1 + __uint_as_float(t1[1])) + (__uint_as_float(t2[1]) + __uint_as_float(t3[1])) + (blo(x1) + blo(c1));
        const __amdgpu_buffer_rsrc_t rout = mkrsrc(a.out, D * 2);
        const unsigned pk = cvtpk(r0, r1);
        __builtin_amdgcn_raw_buffer_store_b16((u16)pk, rout, lane < 4 ? col * 2 : OOB, 0, 0);
        __builtin_amdgcn_raw_buffer_store_b16((u16)(pk >> 16), rout, lane < 4 ? col * 2 + 128 : OOB, 0, 0);
        if (cid == 0) __builtin_amdgcn_raw_buffer_store_b32(tag, rws, lane == 0 ? WS_EPOCH : OOB, 0, AUX_SC1);
      }
    }
  }
}

extern "C" void kernel_launch(void* const* d_in, const int* in_sizes, int n_in, void* d_out, int out_size, void* d_ws, size_t ws_size, hipStream_t stream) {
  Args a;
  a.x = (const u16*)d_in[0]; a.kc = (const u16*)d_in[1]; a.vc = (const u16*)d_in[2]; a.wq = (const u16*)d_in[3]; a.wo = (const u16*)d_in[4];
  a.w1 = (const u16*)d_in[6]; a.b1 = (const u16*)d_in[7]; a.w2 = (const u16*)d_in[8]; a.b2 = (const u16*)d_in[9];
  a.out = (u16*)d_out; a.ws = (unsigned char*)d_ws;
  fused_decode<<<NWG, NTHR, 0, stream>>>(a);
}
```

```cpp
#include <hip/hip_runtime.h>
#include <hip/hip_bf16.h>
#include <stdint.h>

typedef unsigned short u16;
typedef unsigned u32x4 __attribute__((ext_vector_type(4)));
typedef unsigned u32x2 __attribute__((ext_vector_type(2)));
typedef float f32x4 __attribute__((ext_vector_type(4)));
typedef short s16x4 __attribute__((ext_vector_type(4)));
typedef __bf16 bf16x2 __attribute__((ext_vector_type(2)));
typedef __bf16 bf16x8 __attribute__((ext_vector_type(8)));
#define LAS __attribute__((address_space(3)))
#define GAS __attribute__((address_space(1)))
#define DI __device__ __forceinline__

constexpr int D = 448, FF = 1792;
constexpr int NWG = 256, NWAVE = 7, NTHR = NWAVE * 64;
constexpr unsigned WS_EPOCH = 1024;
constexpr unsigned WS_Q = 4096;
constexpr unsigned WS_H = WS_Q + 8 * 16384;
constexpr unsigned WS_L = WS_H + 256 * 256;
constexpr unsigned WS_O = WS_L + 8 * 4096;
constexpr unsigned WS_OUT = WS_O + 8 * 65536;
constexpr unsigned WS_END = WS_OUT + 256 * 2048;
constexpr unsigned WS_STAMP = 8u << 20;
constexpr unsigned SPIN_CAP = 1u << 16;
constexpr unsigned OOB = 0x80000000u;
constexpr int AUX_SC1 = 16;
constexpr float CS = 0.0625f * 1.4426950408889634f;
constexpr unsigned L_WQ = 0, L_X = 14336, L_TAG = 15360, L_K = 16384, L_V = 49152, L_W1 = 81920, L_WO = 89088, L_W2 = 103424, L_MISC = 110592;
constexpr unsigned L_CNT = L_MISC;
constexpr unsigned L_Z0 = L_MISC + 256;
constexpr unsigned L_QF = L_Z0, L_OF = L_Z0 + 16, L_HF = L_Z0 + 32, L_RF = L_Z0 + 48;
constexpr unsigned L_M = L_Z0 + 64, L_LS = L_Z0 + 96;
constexpr unsigned L_WT = L_Z0 + 128;
constexpr unsigned L_OB = L_Z0 + 256;
constexpr unsigned L_ZERO = L_Z0 + 512;
constexpr unsigned L_XN = L_Z0 + 1024;
constexpr unsigned L_QB = L_XN + 5120;
constexpr unsigned L_PB = L_QB + 1152;
constexpr unsigned L_RED = L_PB + 384;
constexpr unsigned L_END = L_RED + 128;
static_assert(L_END <= 163840, "lds");
#define EXP 0
#define WA 20
#define WB 20
#define WC 32
#define QSTART 12
#define LPRIO 1
#define CPRIO 3
#define OSL0 6
#define OSL 5
#define LSTART 64
#define WARM 1
#define LSEP 1
#define QPAD 0
#define QOFF "2048"
#define W1POS 0
#define FSL0 55
#define FSL 5
#define HSTART 44
#define LSL 5
#define NTQ "nt"
#define NTO "nt"
static_assert(WA >= 8 && WA % 4 == 0 && WB % 4 == 0 && WC % 4 == 0 && WA <= 60 && WB <= 60 && WC <= 60, "window");
constexpr int C_K = W1POS ? 16 : 24, C_W1 = W1POS ? 88 : 24;

DI unsigned cvtpk(float lo, float hi) { bf16x2 r; r.x = (__bf16)lo; r.y = (__bf16)hi; return __builtin_bit_cast(unsigned, r); }
DI float rbf(float f) { return __uint_as_float(cvtpk(f, f) & 0xffff0000u); }
DI float blo(unsigned v) { return __uint_as_float(v << 16); }
DI float bhi(unsigned v) { return __uint_as_float(v & 0xffff0000u); }
template <int CTRL> DI float dpp(float x) { return __builtin_bit_cast(float, __builtin_amdgcn_mov_dpp(__builtin_bit_cast(int, x), CTRL, 0xf, 0xf, true)); }
constexpr int ROR1 = 0x121, ROR2 = 0x122, ROR4 = 0x124, ROR8 = 0x128;
DI float swap16_add(float a) { auto s = __builtin_amdgcn_permlane16_swap(__float_as_uint(a), __float_as_uint(a), false, false); return __uint_as_float(s[0]) + __uint_as_float(s[1]); }
DI float swap32_add(float a) { auto s = __builtin_amdgcn_permlane32_swap(__float_as_uint(a), __float_as_uint(a), false, false); return __uint_as_float(s[0]) + __uint_as_float(s[1]); }
DI float swap16_max(float a) { auto s = __builtin_amdgcn_permlane16_swap(__float_as_uint(a), __float_as_uint(a), false, false); return fmaxf(__uint_as_float(s[0]), __uint_as_float(s[1])); }
DI float swap32_max(float a) { auto s = __builtin_amdgcn_permlane32_swap(__float_as_uint(a), __float_as_uint(a), false, false); return fmaxf(__uint_as_float(s[0]), __uint_as_float(s[1])); }
DI float wave_sum(float x) { x += dpp<ROR8>(x); x += dpp<ROR4>(x); x += dpp<ROR2>(x); x += dpp<ROR1>(x); return swap32_add(swap16_add(x)); }
DI __amdgpu_buffer_rsrc_t mkrsrc(const void* p, unsigned bytes) { return __builtin_amdgcn_make_buffer_rsrc(const_cast<void*>(p), 0, bytes, 0x00020000); }
#define BL128(r, off, aux) __builtin_bit_cast(u32x4, __builtin_amdgcn_raw_buffer_load_b128((r), (off), 0, (aux)))
#define BS128(v, r, off, aux) __builtin_amdgcn_raw_buffer_store_b128((v), (r), (off), 0, (aux))
#define CFENCE() asm volatile("" ::: "memory")
#define SB() __builtin_amdgcn_sched_barrier(0)
DI unsigned rfl(unsigned v) { return __builtin_amdgcn_readfirstlane(v); }
DI unsigned ld32(unsigned a) { return *(LAS unsigned*)(size_t)a; }
DI u32x2 ld64(unsigned a) { return *(LAS u32x2*)(size_t)a; }
DI u32x4 ld128(unsigned a) { return *(LAS u32x4*)(size_t)a; }
DI void st32(unsigned a, unsigned v) { *(LAS unsigned*)(size_t)a = v; }
DI void st64(unsigned a, u32x2 v) { *(LAS u32x2*)(size_t)a = v; }
DI void st128(unsigned a, u32x4 v) { *(LAS u32x4*)(size_t)a = v; }
DI u32x2 ldtr(unsigned a) { return __builtin_bit_cast(u32x2, __builtin_amdgcn_ds_read_tr16_b64_v4i16((LAS s16x4*)(size_t)a)); }
DI u32x4 ldtr2(unsigned a, unsigned b) { u32x2 x = ldtr(a), y = ldtr(b); return u32x4{x[0], x[1], y[0], y[1]}; }
DI f32x4 mfma(u32x4 a, u32x4 b, f32x4 c) { return __builtin_amdgcn_mfma_f32_16x16x32_bf16(__builtin_bit_cast(bf16x8, a), __builtin_bit_cast(bf16x8, b), c, 0, 0, 0); }
DI unsigned tagbad(const u32x4& v, unsigned tag) { return (v[1] ^ tag) | (v[3] ^ tag); }

#define STAMP(i) do {} while (0)
#define SPINS(n) do {} while (0)

DI void layernorm8(float (&v)[8], bool valid) {
  const float piv = __builtin_bit_cast(float, __builtin_amdgcn_readfirstlane(__builtin_bit_cast(int, v[0])));
  float s1 = 0.f, s2 = 0.f;
#pragma unroll
  for (int i = 0; i < 8; ++i) { const float d = v[i] - piv; s1 += d; s2 = fmaf(d, d, s2); }
  s1 = valid ? s1 : 0.f; s2 = valid ? s2 : 0.f;
  s1 = wave_sum(s1); s2 = wave_sum(s2);
  const float md = s1 * (1.f / D);
  const float var = fmaxf(s2 * (1.f / D) - md * md, 0.f);
  const float m = rbf(piv + md);
  const float den = rbf(__builtin_amdgcn_sqrtf(rbf(rbf(var) + 1e-5f)));
  const float inv = 1.0f / den;
#pragma unroll
  for (int i = 0; i < 8; ++i) v[i] = rbf(rbf(v[i] - m) * inv);
}

struct Args {
  const u16* x; const u16* wq; const u16* kc; const u16* vc; const u16* w1; const u16* wo; const u16* w2; unsigned char* ws;
  const u16* b1; const u16* b2; u16* out;
};

#define PIN(x) asm volatile("" : "+v"(x))
template <int SLEEP> DI void wait_ge(unsigned a, int need) {
  unsigned t, sv, cap;
  asm volatile("s_mov_b32 %[cap], %[C]\n1:\n\tds_read_b32 %[t], %[a]\n\ts_waitcnt lgkmcnt(0)\n\tv_readfirstlane_b32 %[sv], %[t]\n\ts_cmp_ge_i32 %[sv], %[n]\n\ts_cbranch_scc1 2f\n\t"
               "s_sleep %[sl]\n\ts_sub_u32 %[cap], %[cap], 1\n\ts_cbranch_scc0 1b\n2:"
               : [t] "=&v"(t), [sv] "=&s"(sv), [cap] "=&s"(cap) : [a] "v"(a), [n] "s"(need), [C] "i"(SPIN_CAP * 8), [sl] "i"(SLEEP) : "memory", "scc");
}
DI unsigned perm(unsigned a, unsigned b, unsigned sel) { return __builtin_amdgcn_perm(a, b, sel); }
DI u32x4 pack24(f32x4 v, unsigned tag) {
  const u32x4 ai = __builtin_bit_cast(u32x4, v);
  return u32x4{perm(ai[0], ai[1], 0x07060503u), perm(ai[1], tag, 0x06050100u), perm(ai[2], ai[3], 0x07060503u), perm(ai[3], tag, 0x06050100u)};
}
DI float odd24(unsigned a, unsigned b) { return __uint_as_float(perm(a, b, 0x0403020cu)); }
#define PCLOB "v200", "v201", "v202", "v203", "v204", "v205", "v206", "v207", "v208", "v209", "v210", "v211", "v212", "v213", "v214", "v215"
#define QISS "buffer_load_dwordx4 v[200:203], %[oa], %[r], 0 offen sc1\n\tbuffer_load_dwordx4 v[204:207], %[oa], %[r], 0 offen offset:" QOFF " sc1\n\t"
DI void poll_q_start(__amdgpu_buffer_rsrc_t r, unsigned oa) { asm volatile(QISS :: [r] "s"(r), [oa] "v"(oa) : "memory", PCLOB); }
DI unsigned poll_q(__amdgpu_buffer_rsrc_t r, unsigned oa, unsigned tt, float& d0, float& d1, float& d2, float& d3) {
  unsigned t, u, cap;
  asm volatile("s_mov_b32 %[cap], %[C]\n"
               "1:\n\ts_waitcnt vmcnt(0)\n\tv_perm_b32 %[t], v201, v203, %[sel]\n\tv_perm_b32 %[u], v205, v207, %[sel]\n\tv_xor_b32 %[t], %[t], %[tt]\n\tv_xor_b32 %[u], %[u], %[tt]\n\tv_or_b32 %[t], %[t], %[u]\n\t"
               "v_cmp_ne_u32 vcc, 0, %[t]\n\ts_cbranch_vccz 2f\n\ts_mov_b64 exec, vcc\n\t" QISS
               "s_sub_u32 %[cap], %[cap], 1\n\ts_cbranch_scc0 1b\n\ts_waitcnt vmcnt(0)\n"
               "2:\n\ts_mov_b64 exec, -1\n\tv_add_f32 %[d0], v200, v204\n\tv_add_f32 %[d2], v202, v206\n\t"
               "v_perm_b32 %[t], v200, v201, %[so]\n\tv_perm_b32 %[u], v204, v205, %[so]\n\tv_add_f32 %[d1], %[t], %[u]\n\t"
               "v_perm_b32 %[t], v202, v203, %[so]\n\tv_perm_b32 %[u], v206, v207, %[so]\n\tv_add_f32 %[d3], %[t], %[u]\n\ts_nop 1"
               : [d0] "=&v"(d0), [d1] "=&v"(d1), [d2] "=&v"(d2), [d3] "=&v"(d3), [t] "=&v"(t), [u] "=&v"(u), [cap] "=&s"(cap)
               : [r] "s"(r), [oa] "v"(oa), [tt] "v"(tt), [sel] "s"(0x05040100u), [so] "s"(0x0403020cu), [C] "i"(SPIN_CAP) : "memory", "scc", "vcc", PCLOB);
  return cap;
}
template <int SL> DI unsigned poll_g(__amdgpu_buffer_rsrc_t r, unsigned oa, unsigned tt, u32x4& d) {
  unsigned t, cap;
  asm volatile("s_mov_b32 %[cap], %[C]\n"
               "1:\n\tbuffer_load_dwordx4 v[200:203], %[oa], %[r], 0 offen sc1\n\ts_waitcnt vmcnt(0)\n\tv_perm_b32 %[t], v201, v203, %[sel]\n\t"
               "v_cmp_ne_u32 vcc, %[tt], %[t]\n\ts_cbranch_vccz 2f\n\ts_mov_b64 exec, vcc\n\ts_sleep %[sl]\n\t"
               "s_sub_u32 %[cap], %[cap], 1\n\ts_cbranch_scc0 1b\n"
               "2:\n\ts_mov_b64 exec, -1\n\tv_mov_b32 %[d0], v200\n\tv_mov_b32 %[d1], v201\n\tv_mov_b32 %[d2], v202\n\tv_mov_b32 %[d3], v203"
               : [d0] "=&v"(d[0]), [d1] "=&v"(d[1]), [d2] "=&v"(d[2]), [d3] "=&v"(d[3]), [t] "=&v"(t), [cap] "=&s"(cap)
               : [r] "s"(r), [oa] "v"(oa), [tt] "v"(tt), [sel] "s"(0x05040100u), [C] "i"(SPIN_CAP), [sl] "i"(SL) : "memory", "scc", "vcc", PCLOB);
  return cap;
}

#define OISS(A) "buffer_load_dwordx4 v[" A "], %[oa], %[r], 0 offen sc1\n\t"
#define OCHK(A, r1, r3, L) "s_waitcnt vmcnt(2)\n\tv_perm_b32 %[t], v" r1 ", v" r3 ", %[sel]\n\tv_cmp_ne_u32 vcc, %[tt], %[t]\n\ts_cbranch_vccz " L "f\n\t" OISS(A)
#define OOUT(L, r0, r1, r2, r3) L ":\n\tv_mov_b32 %[d0], v" r0 "\n\tv_perm_b32 %[d1], v" r0 ", v" r1 ", %[so]\n\tv_mov_b32 %[d2], v" r2 "\n\tv_perm_b32 %[d3], v" r2 ", v" r3 ", %[so]\n\t"
DI void poll_o_start(__amdgpu_buffer_rsrc_t r, unsigned oa) {
  asm volatile("s_sleep %[s0]\n\t" OISS("200:203") "s_sleep %[sl]\n\t" OISS("204:207") "s_sleep %[sl]\n\t" OISS("208:211") :: [r] "s"(r), [oa] "v"(oa), [s0] "i"(OSL0), [sl] "i"(OSL) : "memory", PCLOB);
}
DI unsigned poll_o(__amdgpu_buffer_rsrc_t r, unsigned oa, unsigned tt, float& d0, float& d1, float& d2, float& d3) {
  unsigned t, cap;
  asm volatile("s_mov_b32 %[cap], %[C]\n"
               "1:\n\t" OCHK("200:203", "201", "203", "2") OCHK("204:207", "205", "207", "3") OCHK("208:211", "209", "211", "4")
               "s_sub_u32 %[cap], %[cap], 1\n\ts_cbranch_scc0 1b\n"
               OOUT("2", "200", "201", "202", "203") "s_branch 9f\n" OOUT("3", "204", "205", "206", "207") "s_branch 9f\n" OOUT("4", "208", "209", "210", "211") "\n9:"
               : [d0] "=&v"(d0), [d1] "=&v"(d1), [d2] "=&v"(d2), [d3] "=&v"(d3), [t] "=&v"(t), [cap] "=&s"(cap)
               : [r] "s"(r), [oa] "v"(oa), [tt] "v"(tt), [sel] "s"(0x05040100u), [so] "s"(0x0403020cu), [C] "i"(SPIN_CAP) : "memory", "scc", "vcc", PCLOB);
  return cap;
}
#define FCLOB "v200", "v201", "v202", "v203", "v204", "v205", "v206", "v207", "v208", "v209", "v210", "v211", "v212", "v213", "v214", "v215", "v216", "v217", "v218", "v219", "v220", "v221", "v222", "v223", "v224", "v225", "v226", "v227", "v228", "v229", "v230", "v231", "v232", "v233", "v234", "v235", "v236", "v237", "v238", "v239", "v240", "v241", "v242", "v243", "v244", "v245", "v246", "v247"
DI unsigned poll_f(__amdgpu_buffer_rsrc_t r, unsigned oa, unsigned tt, float& d0, float& d1, float& d2, float& d3) {
  unsigned t, u, y, z, cap;
  asm volatile("s_mov_b32 %[cap], %[C]\n\t"
               "buffer_load_dwordx4 v[200:203], %[oa], %[r], 0 offen sc1\n\tbuffer_load_dwordx4 v[204:207], %[oa], %[r], %[k1] offen sc1\n\tbuffer_load_dwordx4 v[208:211], %[oa], %[r], %[k2] offen sc1\n\tbuffer_load_dwordx4 v[212:215], %[oa], %[r], %[k3] offen sc1\n\t" "s_sleep %[sl]\n\t"
               "buffer_load_dwordx4 v[216:219], %[oa], %[r], 0 offen sc1\n\tbuffer_load_dwordx4 v[220:223], %[oa], %[r], %[k1] offen sc1\n\tbuffer_load_dwordx4 v[224:227], %[oa], %[r], %[k2] offen sc1\n\tbuffer_load_dwordx4 v[228:231], %[oa], %[r], %[k3] offen sc1\n\t" "s_sleep %[sl]\n\t"
               "buffer_load_dwordx4 v[232:235], %[oa], %[r], 0 offen sc1\n\tbuffer_load_dwordx4 v[236:239], %[oa], %[r], %[k1] offen sc1\n\tbuffer_load_dwordx4 v[240:243], %[oa], %[r], %[k2] offen sc1\n\tbuffer_load_dwordx4 v[244:247], %[oa], %[r], %[k3] offen sc1\n\t"
               "\n1:\n\t"
               "s_waitcnt vmcnt(8)\n\tv_perm_b32 %[t], v201, v203, %[sel]\n\tv_perm_b32 %[u], v205, v207, %[sel]\n\tv_perm_b32 %[y], v209, v211, %[sel]\n\tv_perm_b32 %[z], v213, v215, %[sel]\n\tv_xor_b32 %[t], %[t], %[tt]\n\tv_xor_b32 %[u], %[u], %[tt]\n\tv_xor_b32 %[y], %[y], %[tt]\n\tv_xor_b32 %[z], %[z], %[tt]\n\tv_or3_b32 %[t], %[t], %[u], %[y]\n\tv_or_b32 %[t], %[t], %[z]\n\tv_cmp_ne_u32 vcc, 0, %[t]\n\tv_perm_b32 v201, v200, v201, %[so]\n\tv_perm_b32 v205, v204, v205, %[so]\n\tv_perm_b32 v209, v208, v209, %[so]\n\tv_perm_b32 v213, v212, v213, %[so]\n\tv_perm_b32 v203, v202, v203, %[so]\n\tv_perm_b32 v207, v206, v207, %[so]\n\tv_perm_b32 v211, v210, v211, %[so]\n\tv_perm_b32 v215, v214, v215, %[so]\n\tv_pk_add_f32 v[200:201], v[200:201], v[204:205]\n\tv_pk_add_f32 v[208:209], v[208:209], v[212:213]\n\tv_pk_add_f32 v[202:203], v[202:203], v[206:207]\n\tv_pk_add_f32 v[210:211], v[210:211], v[214:215]\n\tv_add_f32 %[d0], v200, v208\n\tv_add_f32 %[d1], v201, v209\n\tv_add_f32 %[d2], v202, v210\n\tv_add_f32 %[d3], v203, v211\n\ts_cbranch_vccz 2f\n\ts_mov_b64 exec, vcc\n\tbuffer_load_dwordx4 v[200:203], %[oa], %[r], 0 offen sc1\n\tbuffer_load_dwordx4 v[204:207], %[oa], %[r], %[k1] offen sc1\n\tbuffer_load_dwordx4 v[208:211], %[oa], %[r], %[k2] offen sc1\n\tbuffer_load_dwordx4 v[212:215], %[oa], %[r], %[k3] offen sc1\n\t"
               "s_waitcnt vmcnt(8)\n\tv_perm_b32 %[t], v217, v219, %[sel]\n\tv_perm_b32 %[u], v221, v223, %[sel]\n\tv_perm_b32 %[y], v225, v227, %[sel]\n\tv_perm_b32 %[z], v229, v231, %[sel]\n\tv_xor_b32 %[t], %[t], %[tt]\n\tv_xor_b32 %[u], %[u], %[tt]\n\tv_xor_b32 %[y], %[y], %[tt]\n\tv_xor_b32 %[z], %[z], %[tt]\n\tv_or3_b32 %[t], %[t], %[u], %[y]\n\tv_or_b32 %[t], %[t], %[z]\n\tv_cmp_ne_u32 vcc, 0, %[t]\n\tv_perm_b32 v217, v216, v217, %[so]\n\tv_perm_b32 v221, v220, v221, %[so]\n\tv_perm_b32 v225, v224, v225, %[so]\n\tv_perm_b32 v229, v228, v229, %[so]\n\tv_perm_b32 v219, v218, v219, %[so]\n\tv_perm_b32 v223, v222, v223, %[so]\n\tv_perm_b32 v227, v226, v227, %[so]\n\tv_perm_b32 v231, v230, v231, %[so]\n\tv_pk_add_f32 v[216:217], v[216:217], v[220:221]\n\tv_pk_add_f32 v[224:225], v[224:225], v[228:229]\n\tv_pk_add_f32 v[218:219], v[218:219], v[222:223]\n\tv_pk_add_f32 v[226:227], v[226:227], v[230:231]\n\tv_add_f32 %[d0], v216, v224\n\tv_add_f32 %[d1], v217, v225\n\tv_add_f32 %[d2], v218, v226\n\tv_add_f32 %[d3], v219, v227\n\ts_cbranch_vccz 2f\n\ts_mov_b64 exec, vcc\n\tbuffer_load_dwordx4 v[216:219], %[oa], %[r], 0 offen sc1\n\tbuffer_load_dwordx4 v[220:223], %[oa], %[r], %[k1] offen sc1\n\tbuffer_load_dwordx4 v[224:227], %[oa], %[r], %[k2] offen sc1\n\tbuffer_load_dwordx4 v[228:231], %[oa], %[r], %[k3] offen sc1\n\t"
               "s_waitcnt vmcnt(8)\n\tv_perm_b32 %[t], v233, v235, %[sel]\n\tv_perm_b32 %[u], v237, v239, %[sel]\n\tv_perm_b32 %[y], v241, v243, %[sel]\n\tv_perm_b32 %[z], v245, v247, %[sel]\n\tv_xor_b32 %[t], %[t], %[tt]\n\tv_xor_b32 %[u], %[u], %[tt]\n\tv_xor_b32 %[y], %[y], %[tt]\n\tv_xor_b32 %[z], %[z], %[tt]\n\tv_or3_b32 %[t], %[t], %[u], %[y]\n\tv_or_b32 %[t], %[t], %[z]\n\tv_cmp_ne_u32 vcc, 0, %[t]\n\tv_perm_b32 v233, v232, v233, %[so]\n\tv_perm_b32 v237, v236, v237, %[so]\n\tv_perm_b32 v241, v240, v241, %[so]\n\tv_perm_b32 v245, v244, v245, %[so]\n\tv_perm_b32 v235, v234, v235, %[so]\n\tv_perm_b32 v239, v238, v239, %[so]\n\tv_perm_b32 v243, v242, v243, %[so]\n\tv_perm_b32 v247, v246, v247, %[so]\n\tv_pk_add_f32 v[232:233], v[232:233], v[236:237]\n\tv_pk_add_f32 v[240:241], v[240:241], v[244:245]\n\tv_pk_add_f32 v[234:235], v[234:235], v[238:239]\n\tv_pk_add_f32 v[242:243], v[242:243], v[246:247]\n\tv_add_f32 %[d0], v232, v240\n\tv_add_f32 %[d1], v233, v241\n\tv_add_f32 %[d2], v234, v242\n\tv_add_f32 %[d3], v235, v243\n\ts_cbranch_vccz 2f\n\ts_mov_b64 exec, vcc\n\tbuffer_load_dwordx4 v[232:235], %[oa], %[r], 0 offen sc1\n\tbuffer_load_dwordx4 v[236:239], %[oa], %[r], %[k1] offen sc1\n\tbuffer_load_dwordx4 v[240:243], %[oa], %[r], %[k2] offen sc1\n\tbuffer_load_dwordx4 v[244:247], %[oa], %[r], %[k3] offen sc1\n\t"
               "s_sub_u32 %[cap], %[cap], 1\n\ts_cbranch_scc0 1b\n"
               "2:\n\ts_mov_b64 exec, -1\n\ts_nop 1"
               : [d0] "=&v"(d0), [d1] "=&v"(d1), [d2] "=&v"(d2), [d3] "=&v"(d3), [t] "=&v"(t), [u] "=&v"(u), [y] "=&v"(y), [z] "=&v"(z), [cap] "=&s"(cap)
               : [r] "s"(r), [oa] "v"(oa), [tt] "v"(tt), [sel] "s"(0x05040100u), [so] "s"(0x0403020cu), [C] "i"(SPIN_CAP), [sl] "i"(FSL), [k1] "s"(131072u), [k2] "s"(262144u), [k3] "s"(393216u) : "memory", "scc", "vcc", FCLOB);
  return cap;
}
#define LISS(A) "buffer_load_dwordx2 v[" A "], %[oa], %[r], 0 offen sc1\n\t"
#define LCHK(A, r0, r1) "s_waitcnt vmcnt(2)\n\tv_cmp_ne_u32 vcc, %[tg], v" r1 "\n\tv_mov_b32 %[d], v" r0 "\n\ts_cbranch_vccz 2f\n\t" LISS(A)
DI unsigned poll_l(__amdgpu_buffer_rsrc_t r, unsigned oa, unsigned tg, float& d) {
  unsigned cap;
  asm volatile("s_mov_b32 %[cap], %[C]\n\t" LISS("248:249") "s_sleep %[sl]\n\t" LISS("250:251") "s_sleep %[sl]\n\t" LISS("252:253")
               "\n1:\n\t" LCHK("248:249", "248", "249") LCHK("250:251", "250", "251") LCHK("252:253", "252", "253")
               "s_sub_u32 %[cap], %[cap], 1\n\ts_cbranch_scc0 1b\n2:\n\ts_nop 1"
               : [d] "=&v"(d), [cap] "=&s"(cap) : [r] "s"(r), [oa] "v"(oa), [tg] "v"(tg), [C] "i"(SPIN_CAP), [sl] "i"(LSL) : "memory", "scc", "vcc", "v248", "v249", "v250", "v251", "v252", "v253");
  return cap;
}

#define FINISH() do {} while (0)

__global__ void __launch_bounds__(NTHR, 2) fused_decode(Args a_) {
  __shared__ __attribute__((aligned(16))) unsigned char smem[L_END];
  const Args& a = a_;
  const unsigned lane = threadIdx.x & 63, wave = rfl(threadIdx.x >> 6);
  const unsigned b = blockIdx.x, g = b & 7, j = b >> 3;
  asm volatile("" :: "s"((unsigned)(size_t)(LAS unsigned char*)smem));

#define LD(NT, r, so, va, off) "buffer_load_dwordx4 " va ", " r ", " so " offen offset:" off " " NT " lds\n\t.set fd_n, fd_n + 1\n\t"
#define G4(NT, r, so, a0, a1, a2, a3) "s_nop 0\n\t" LD(NT, r, so, a0, "0") LD(NT, r, so, a1, "1024") LD(NT, r, so, a2, "2048") LD(NT, r, so, a3, "3072")
#define M0N "s_add_u32 m0, m0, 0x1000\n\t"
#define PUB(w) ".rept 16\n\t.if (fd_n - fd_p) > (" w " - 4)\n\t.if fd_p == 0\n\ts_waitcnt vmcnt(fd_n - 2)\n\tv_mov_b32 v17, 2\n\tds_write_b32 v16, v17\n\t.endif\n\t" \
      ".set fd_p, fd_p + 4\n\tv_mov_b32 v17, fd_p\n\ts_waitcnt vmcnt(fd_n - fd_p)\n\tds_write_b32 v16, v17\n\t.endif\n\t.endr\n\t"
#define KV(r, w) ".rept 4\n\t" G4("nt", r, "s59", "v8", "v9", "v10", "v11") M0N "s_add_u32 s59, s59, 0x1000\n\t" PUB(w) G4("nt", r, "s59", "v12", "v13", "v14", "v15") M0N "s_add_u32 s59, s59, 0x1000\n\t" PUB(w) ".endr\n\t"
#define W1(w) "s_mov_b32 m0, %[l1]\n\t" G4(NTO, "s[80:83]", "s58", "v4", "v5", "v6", "v7") M0N "s_add_u32 s58, s58, 0x1c000\n\t" PUB(w) \
      "s_nop 0\n\t" LD(NTO, "s[80:83]", "s58", "v4", "0") LD(NTO, "s[80:83]", "s58", "v5", "1024") LD(NTO, "s[80:83]", "s58", "v6", "2048") \
      "s_mov_b32 m0, %[lo]\n\ts_nop 0\n\t" LD(NTO, "s[84:87]", "s63", "v18", "0") PUB(w)
#define LST(i)
#define LEND "s_endpgm\n"
  asm volatile(
      "s_cmp_lg_u32 %[w], 0\n\ts_cbranch_scc1 9f\n\t"
      "s_load_dwordx2 s[64:65], %[ka], 0\n\ts_load_dwordx2 s[68:69], %[ka], 8\n\ts_load_dwordx2 s[92:93], %[ka], 56\n\ts_load_dwordx2 s[72:73], %[ka], 16\n\ts_load_dwordx2 s[76:77], %[ka], 24\n\t"
      "s_setprio %c[lp]\n\t.set fd_n, 0\n\t.set fd_p, 0\n\t"
      "s_lshr_b32 s57, %[b], 3\n\ts_and_b32 s56, %[b], 7\n\t"
      "s_lshl_b32 s58, s56, 10\n\ts_lshr_b32 s59, s57, 2\n\ts_lshl_b32 s59, s59, 7\n\ts_add_u32 s58, s58, s59\n\ts_and_b32 s59, s57, 3\n\ts_mul_i32 s59, s59, 0xe0000\n\ts_add_u32 s58, s58, s59\n\t"
      "s_movk_i32 s66, 896\n\ts_mov_b32 s67, 0x20000\n\ts_mov_b32 s70, %[qb]\n\ts_mov_b32 s71, s67\n\ts_movk_i32 s94, 2048\n\ts_mov_b32 s95, s67\n\t"
      "v_lshlrev_b32 v1, 4, %[t]\n\tv_lshlrev_b32 v2, 1, %[t]\n\tv_and_b32 v3, 0x70, v1\n\tv_and_b32 v2, 0x60, v2\n\tv_xor_b32 v18, v1, v2\n\tv_xor_b32 v3, v3, v2\n\tv_lshrrev_b32 v2, 3, %[t]\n\t"
      "v_lshl_add_u32 v4, v2, 13, v3\n\tv_add_u32 v5, 0xfc00, v4\n\tv_add_u32 v6, 0xfc00, v5\n\tv_add_u32 v7, 0xfc00, v6\n\t"
      "v_lshlrev_b32 v16, 2, %[t]\n\tv_add_u32 v16, %[lc], v16\n\tv_mov_b32 v17, 0\n\t"
      "s_mov_b32 m0, %[lx]\n\tds_write_b32 v16, v17\n\ts_waitcnt lgkmcnt(0)\n\t" LST(5)
      LD("", "s[64:67]", "0", "v1", "0")
      "buffer_load_dword off, s[92:95], 0 offset:1024 sc1 lds\n\t.set fd_n, fd_n + 1\n\t"
      "s_mov_b32 m0, 0\n\t"
      ".rept 3\n\t" G4(NTQ, "s[68:71]", "s58", "v4", "v5", "v6", "v7") M0N "s_add_u32 s58, s58, 0x40000\n\t.endr\n\t"
      "s_nop 0\n\t" LD(NTQ, "s[68:71]", "s58", "v4", "0") LD(NTQ, "s[68:71]", "s58", "v5", "1024") LST(1)
      "s_barrier\n\t"
      "s_load_dwordx2 s[80:81], %[ka], 32\n\ts_load_dwordx2 s[84:85], %[ka], 40\n\ts_load_dwordx2 s[88:89], %[ka], 48\n\t"
      "s_mov_b32 s74, 0x2000000\n\ts_mov_b32 s75, s67\n\ts_mov_b32 s78, s74\n\ts_mov_b32 s79, s67\n\t"
      "s_lshl_b32 s60, s56, 22\n\ts_add_u32 s60, s60, 0x300000\n\ts_lshl_b32 s59, s57, 15\n\ts_add_u32 s59, s59, s60\n\ts_mov_b32 s60, s59\n\t"
      "v_lshrrev_b32 v9, 1, %[t]\n\tv_and_b32 v9, 16, v9\n\tv_xor_b32 v8, v1, v9\n\tv_xor_b32 v9, 32, v8\n\tv_xor_b32 v10, 64, v8\n\tv_xor_b32 v11, 0x60, v8\n\t"
      "v_xor_b32 v12, 0x80, v8\n\tv_xor_b32 v13, 0xa0, v8\n\tv_xor_b32 v14, 0xc0, v8\n\tv_xor_b32 v15, 0xe0, v8\n\t"
      "s_mul_i32 s61, s56, 28\n\ts_add_u32 s61, s61, s57\n\ts_lshr_b32 s58, s61, 3\n\ts_lshl_b32 s58, s58, 7\n\ts_and_b32 s63, s61, 7\n\ts_mul_i32 s63, s63, 0x31000\n\ts_add_u32 s58, s58, s63\n\t"
      "s_cmp_lt_u32 s57, 28\n\ts_cselect_b32 s82, %[fb], 0\n\ts_mov_b32 s83, s67\n\ts_mov_b32 s90, s82\n\ts_mov_b32 s91, s67\n\ts_mov_b32 s86, s70\n\ts_mov_b32 s87, s67\n\t"
      "s_movk_i32 s62, 0xe00\n\tv_mad_u32_u24 v4, v2, s62, v3\n\tv_add_u32 v5, 0x6c00, v4\n\tv_add_u32 v6, 0x6c00, v5\n\tv_add_u32 v7, 0x6c00, v6\n\t"
      "s_lshl_b32 s63, s56, 5\n\ts_add_u32 s63, s63, s57\n\ts_mul_i32 s63, s63, 0x3800\n\ts_mul_i32 s61, s61, 0x1c00\n\ts_waitcnt lgkmcnt(0)\n\t"
      PUB("%c[wa]")
      W1("%c[wa]")
      "s_mov_b32 m0, %[lk]\n\t"
      KV("s[72:75]", "%c[wa]") LST(2)
      "s_mov_b32 s59, s60\n\t"
      KV("s[76:79]", "%c[wb]") LST(3)
      "s_mov_b32 m0, %[lo] + 0x400\n\ts_add_u32 s59, s63, 0x400\n\t"
      ".rept 3\n\t" G4(NTO, "s[84:87]", "s59", "v18", "v18", "v18", "v18") M0N "s_add_u32 s59, s59, 0x1000\n\t" PUB("%c[wc]") ".endr\n\t"
      "s_nop 0\n\t" LD(NTO, "s[84:87]", "s59", "v18", "0") "s_add_u32 m0, m0, 0x400\n\ts_nop 0\n\t"
      LD(NTO, "s[88:91]", "s61", "v18", "0") LD(NTO, "s[88:91]", "s61", "v18", "1024") LD(NTO, "s[88:91]", "s61", "v18", "2048") "s_add_u32 m0, m0, 0xc00\n\ts_add_u32 s61, s61, 0xc00\n\t" PUB("%c[wc]")
      G4(NTO, "s[88:91]", "s61", "v18", "v18", "v18", "v18") LST(6)
      PUB("4") PUB("4") LST(4) LEND
      "9:"
      :
      : [b] "s"(b), [w] "s"(wave), [t] "v"(threadIdx.x), [ka] "s"(__builtin_amdgcn_kernarg_segment_ptr()), [fb] "i"(448u * 3584u), [qb] "i"(448u * 8192u),
        [lp] "i"(LPRIO), [lc] "i"(L_CNT), [lx] "i"(L_X), [lo] "i"(L_WO), [lk] "i"(L_K), [l1] "i"(L_W1), [wa] "i"(WA), [wb] "i"(WB), [wc] "i"(WC)
      : "memory", "scc", "s56", "s57", "s58", "s59", "s60", "s61", "s62", "s63", "s64", "s65", "s66", "s67", "s68", "s69", "s70", "s71", "s72", "s73", "s74", "s75", "s76", "s77", "s78", "s79",
        "s80", "s81", "s82", "s83", "s84", "s85", "s86", "s87", "s88", "s89", "s90", "s91", "s92", "s93", "s94", "s95",
        "v1", "v2", "v3", "v4", "v5", "v6", "v7", "v8", "v9", "v10", "v11", "v12", "v13", "v14", "v15", "v16", "v17", "v18");
  const unsigned grp = lane >> 4, n = lane & 15, q = n >> 2, p = lane & 3;
  if (__builtin_expect(wave >= 1 && wave < 5, 1)) {
    const unsigned w = wave - 1;
    __builtin_amdgcn_s_setprio(CPRIO);
    const __amdgpu_buffer_rsrc_t rws = mkrsrc(a.ws, WS_END);
    st32(L_Z0 + w * 256 + lane * 4, 0u);
    const unsigned krow = 16 * (grp >> 1) + 4 * (grp & 1) + q;
    unsigned acnt = L_CNT;
    unsigned xl = L_X + lane * 16;
    unsigned xa = L_XN + w * 1024 + (lane >> 2) * 64 + (4 * ((lane >> 1) & 1) + (lane & 1)) * 8;
    unsigned xra = L_XN + w * 1024 + (j & 3) * 224 + grp * 16, xrb = grp < 2 ? xra + 192 : L_ZERO;
    unsigned pa = L_WQ + krow * 128 + ((w ^ ((krow >> 1) & 3)) << 5) + p * 8, pb = (pa & ~2048u) + 12288;
    unsigned po = (n == 0 ? 0u : OOB) | (QPAD ? WS_Q + g * 16384 + (j & 2) * 4096 + (j >> 3) * 2048 + (j & 1) * 1024 + ((j >> 2) & 1) * 512 + w * 128 + grp * 16 : WS_Q + g * 16384 + (j & 3) * 2048 + ((j >> 2) * 16 + 4 * w + grp) * 16);
    unsigned qo = QPAD ? WS_Q + g * 16384 + (lane >> 5) * 8192 + w * 2048 + ((lane >> 2) & 7) * 128 + p * 16 : WS_Q + g * 16384 + (lane >> 5) * 4096 + w * 512 + (lane & 31) * 16;
    unsigned qwa = L_QB + (w >> 1) * 576 + (w & 1) * 256 + (lane & 31) * 8, fwa = L_QF + w * 4;
    unsigned ka[8];
#pragma unroll
    for (int ks = 0; ks < 8; ++ks) { ka[ks] = L_K + (16 * w + n) * 512 + (((4 * ks + grp) ^ n) << 4); PIN(ka[ks]); }
    unsigned qra = L_QB + (n & 1) * 576 + grp * 16;
    unsigned mwa = L_M + (n & 1) * 16 + w * 4, mra = L_M + (n & 1) * 16;
    unsigned pwa = L_PB + (n & 1) * 192 + (w >> 1) * 64 + (2 * (w & 1) + (grp >> 1)) * 16 + (grp & 1) * 4;
    unsigned pr[4], va[4], vb[4];
    const unsigned vr = 8 * (grp & 1) + 2 * q;
#pragma unroll
    for (int t = 0; t < 4; ++t) {
      pr[t] = ((n >> 1) & 3) == (unsigned)t ? L_PB + (n & 1) * 192 + grp * 16 : L_ZERO; PIN(pr[t]);
      const unsigned c16 = 8 * w + 2 * t + (p >> 1);
      va[t] = L_V + (16 * (grp >> 1) + vr) * 512 + ((c16 ^ vr) << 4) + (p & 1) * 8; PIN(va[t]);
      vb[t] = (va[t] ^ 16) + 512; PIN(vb[t]);
    }
    unsigned lo = ((w == 0 && lane < 2) ? 0u : OOB) | (WS_L + g * 4096 + (LSEP ? j * 128 + lane * 8 : lane * 256 + j * 8));
    unsigned so = (n < 8 ? 0u : OOB) | (WS_O + g * 65536 + j * 2048 + w * 512 + (n & 1) * 256 + (n >> 1) * 64 + grp * 16);
    unsigned og = WS_O + g * 65536 + (8 * w + ((lane >> 2) & 7)) * 2048 + ((j >> 2) & 3) * 512 + (j >> 4) * 256 + (j & 3) * 64 + p * 16;
    unsigned wta = L_WT + (8 * w + ((lane >> 2) & 7)) * 4;
    unsigned owa = L_OB + w * 64 + p * 16;
    unsigned wa[7], wb[7];
#pragma unroll
    for (int t = 0; t < 7; ++t) {
      unsigned o = (4 * grp + q) * 896 + w * 32 + p * 8 + t * 128; o ^= ((o >> 8) & 3) << 5; wa[t] = o + L_WO; PIN(wa[t]);
      wb[t] = (grp < 2 && j < 28) ? wa[t] + 14336 : wa[t]; PIN(wb[t]);
    }
    unsigned aa[4];
#pragma unroll
    for (int v = 0; v < 4; ++v) { aa[v] = q == (unsigned)v ? L_OB + (n & 3) * 64 + grp * 16 : L_ZERO; PIN(aa[v]); }
    unsigned oo = ((lane & 1) ? OOB : 0u) | (WS_OUT + b * 2048 + w * 512 + lane * 8);
    PIN(acnt); PIN(xl); PIN(xa); PIN(xra); PIN(xrb); PIN(pa); PIN(pb); PIN(po); PIN(qo); PIN(qwa); PIN(fwa); PIN(qra); PIN(mwa); PIN(mra); PIN(pwa); PIN(lo); PIN(so); PIN(og); PIN(wta); PIN(owa); PIN(oo);
    asm volatile("s_waitcnt lgkmcnt(0)\n\ts_barrier" ::: "memory");
    unsigned tag;
    {
      wait_ge<0>(acnt, 2);
      const u32x4 xr = ld128(xl);
      tag = ld32(xl + 1024 - lane * 16) + 1u;
      float xv[8];
#pragma unroll
      for (int i = 0; i < 4; ++i) { xv[2 * i] = blo(xr[i]); xv[2 * i + 1] = bhi(xr[i]); }
      layernorm8(xv, lane < 56);
      st64(xa, u32x2{perm(__float_as_uint(xv[1]), __float_as_uint(xv[0]), 0x07060302u), perm(__float_as_uint(xv[3]), __float_as_uint(xv[2]), 0x07060302u)});
      st64(xa + 16, u32x2{perm(__float_as_uint(xv[5]), __float_as_uint(xv[4]), 0x07060302u), perm(__float_as_uint(xv[7]), __float_as_uint(xv[6]), 0x07060302u)});
      CFENCE();
      u32x4 xf[4];
#pragma unroll
      for (int ks = 0; ks < 3; ++ks) xf[ks] = ld128(xra + ks * 64);
      xf[3] = ld128(xrb);
#pragma unroll
      for (int ks = 0; ks < 4; ++ks) PIN(xf[ks]);
      PIN(tag);
      STAMP(1);
      wait_ge<0>(acnt, 12);
      f32x4 acc = {0.f, 0.f, 0.f, 0.f}, acc2 = acc;
      { const u32x4 w0 = ldtr2(pa, pa + 1024), w1 = ldtr2(pa + 4096, pa + 5120);
        acc = mfma(w0, xf[0], acc); acc2 = mfma(w1, xf[1], acc2); }
      PIN(acc); PIN(acc2);
      u32x2 wl0, wl1, wl2, wl3;
      { unsigned t, sv, cap;
        asm volatile("s_mov_b32 %[cap], %[C]\n1:\n\tds_read_b32 %[t], %[a]\n\tds_read_b64_tr_b16 %[w0], %[pa] offset:8192\n\tds_read_b64_tr_b16 %[w1], %[pa] offset:9216\n\t"
                     "ds_read_b64_tr_b16 %[w2], %[pb]\n\tds_read_b64_tr_b16 %[w3], %[pb] offset:1024\n\ts_waitcnt lgkmcnt(4)\n\t"
                     "v_readfirstlane_b32 %[sv], %[t]\n\ts_cmp_ge_i32 %[sv], 16\n\ts_cbranch_scc1 2f\n\ts_sub_u32 %[cap], %[cap], 1\n\ts_cbranch_scc0 1b\n2:\n\ts_waitcnt lgkmcnt(0)"
                     : [t] "=&v"(t), [sv] "=&s"(sv), [cap] "=&s"(cap), [w0] "=&v"(wl0), [w1] "=&v"(wl1), [w2] "=&v"(wl2), [w3] "=&v"(wl3) : [a] "v"(acnt), [pa] "v"(pa), [pb] "v"(pb), [C] "i"(SPIN_CAP * 8) : "memory", "scc"); }
      STAMP(2);
      acc = mfma(u32x4{wl0[0], wl0[1], wl1[0], wl1[1]}, xf[2], acc); acc2 = mfma(u32x4{wl2[0], wl2[1], wl3[0], wl3[1]}, xf[3], acc2);
      BS128(pack24(acc + acc2, tag), rws, po, AUX_SC1);
      STAMP(3);
    }
    const unsigned tt = perm(tag, tag, 0x01000100u);
    if (EXP & 8) asm volatile("s_endpgm");
    if (WARM & 1) asm volatile("buffer_load_dword v200, %0, %1, 0 offen sc1" :: "v"(og), "s"(rws) : "memory", PCLOB);
    u32x4 kf[8];
    {
      wait_ge<1>(acnt, C_K + QSTART);
      poll_q_start(rws, qo);
      wait_ge<0>(acnt, C_K + 8 + 8 * w);
#pragma unroll
      for (int ks = 0; ks < 8; ++ks) kf[ks] = ld128(ka[ks]);
#pragma unroll
      for (int ks = 0; ks < 8; ++ks) PIN(kf[ks]);
      float d0, d1, d2, d3;
      const unsigned cap = poll_q(rws, qo, tt, d0, d1, d2, d3);
      SPINS(SPIN_CAP - cap);
      st64(qwa, u32x2{cvtpk(swap32_add(d0), swap32_add(d1)), cvtpk(swap32_add(d2), swap32_add(d3))});
      CFENCE();
      st32(fwa, 1u);
      STAMP(4);
    }
    float M;
    {
      u32x4 qf[8];
#pragma nounroll
      for (unsigned spin = 0;; ++spin) {
        u32x4 fl;
        asm volatile("ds_read_b128 %[fl], %[a] offset:%c[qf]\n\t"
                     "ds_read_b128 %[q0], %[qa]\n\tds_read_b128 %[q1], %[qa] offset:64\n\tds_read_b128 %[q2], %[qa] offset:128\n\tds_read_b128 %[q3], %[qa] offset:192\n\t"
                     "ds_read_b128 %[q4], %[qa] offset:256\n\tds_read_b128 %[q5], %[qa] offset:320\n\tds_read_b128 %[q6], %[qa] offset:384\n\tds_read_b128 %[q7], %[qa] offset:448\n\ts_waitcnt lgkmcnt(0)"
                     : [fl] "=&v"(fl), [q0] "=&v"(qf[0]), [q1] "=&v"(qf[1]), [q2] "=&v"(qf[2]), [q3] "=&v"(qf[3]), [q4] "=&v"(qf[4]), [q5] "=&v"(qf[5]), [q6] "=&v"(qf[6]), [q7] "=&v"(qf[7])
                     : [a] "v"(acnt), [qa] "v"(qra), [qf] "i"(L_QF - L_CNT) : "memory");
        if (rfl(fl[0] & fl[1] & fl[2] & fl[3]) || spin > SPIN_CAP * 8) break;
        __builtin_amdgcn_s_sleep(1);
      }
      STAMP(5);
      f32x4 s0 = {0.f, 0.f, 0.f, 0.f}, s1 = s0;
#pragma unroll
      for (int ks = 0; ks < 4; ++ks) { s0 = mfma(kf[2 * ks], qf[2 * ks], s0); s1 = mfma(kf[2 * ks + 1], qf[2 * ks + 1], s1); }
      s0 += s1;
      const unsigned c0 = cvtpk(s0[0], s0[1]), c1 = cvtpk(s0[2], s0[3]);
      const float r0 = blo(c0) * CS, r1 = bhi(c0) * CS, r2 = blo(c1) * CS, r3 = bhi(c1) * CS;
      const float m = swap32_max(swap16_max(fmaxf(fmaxf(r0, r1), fmaxf(r2, r3))));
      st32(mwa, __float_as_uint(m) | 1u);
      u32x4 ma;
#pragma nounroll
      for (unsigned spin = 0;; ++spin) {
        asm volatile("ds_read_b128 %[ma], %[mr]\n\ts_waitcnt lgkmcnt(0)" : [ma] "=&v"(ma) : [mr] "v"(mra) : "memory");
        if (!__any((ma[0] & ma[1] & ma[2] & ma[3] & 1u) == 0) || spin > SPIN_CAP * 8) break;
      }
      M = fmaxf(fmaxf(__uint_as_float(ma[0]), __uint_as_float(ma[1])), fmaxf(__uint_as_float(ma[2]), __uint_as_float(ma[3])));
      const float p0 = __builtin_amdgcn_exp2f(r0 - M), p1 = __builtin_amdgcn_exp2f(r1 - M), p2 = __builtin_amdgcn_exp2f(r2 - M), p3 = __builtin_amdgcn_exp2f(r3 - M);
      st32(pwa, cvtpk(p0, p2)); st32(pwa + 8, cvtpk(p1, p3));
      CFENCE();
      const float l = swap32_add(swap16_add((p0 + p1) + (p2 + p3)));
      st32(mwa + (L_LS - L_M), __float_as_uint(l) | 1u);
      STAMP(6);
    }
    {
      u32x4 pf[8], vf[4], la;
      wait_ge<0>(acnt, C_K + 48);
#pragma unroll
      for (int t = 0; t < 4; ++t) vf[t] = ldtr2(va[t], vb[t]);
#pragma nounroll
      for (unsigned spin = 0;; ++spin) {
        asm volatile("ds_read_b128 %[fl], %[a] offset:%c[qf]\n\t"
                     "ds_read_b128 %[q0], %[a0]\n\tds_read_b128 %[q1], %[a0] offset:64\n\tds_read_b128 %[q2], %[a1]\n\tds_read_b128 %[q3], %[a1] offset:64\n\t"
                     "ds_read_b128 %[q4], %[a2]\n\tds_read_b128 %[q5], %[a2] offset:64\n\tds_read_b128 %[q6], %[a3]\n\tds_read_b128 %[q7], %[a3] offset:64\n\ts_waitcnt lgkmcnt(0)"
                     : [fl] "=&v"(la), [q0] "=&v"(pf[0]), [q1] "=&v"(pf[1]), [q2] "=&v"(pf[2]), [q3] "=&v"(pf[3]), [q4] "=&v"(pf[4]), [q5] "=&v"(pf[5]), [q6] "=&v"(pf[6]), [q7] "=&v"(pf[7])
                     : [a] "v"(mra), [a0] "v"(pr[0]), [a1] "v"(pr[1]), [a2] "v"(pr[2]), [a3] "v"(pr[3]), [qf] "i"(L_LS - L_M) : "memory");
        if (!__any((la[0] & la[1] & la[2] & la[3] & 1u) == 0) || spin > SPIN_CAP * 8) break;
      }
      f32x4 o0 = {0.f, 0.f, 0.f, 0.f}, o1 = o0;
      o0 = mfma(vf[0], pf[0], o0); o1 = mfma(vf[1], pf[2], o1); o0 = mfma(vf[2], pf[4], o0); o1 = mfma(vf[3], pf[6], o1);
      const float L = (__uint_as_float(la[0]) + __uint_as_float(la[1])) + (__uint_as_float(la[2]) + __uint_as_float(la[3]));
      const u32x2 gm = {__float_as_uint(M + __builtin_amdgcn_logf(L)), tag};
      if (w == 0) __builtin_amdgcn_raw_buffer_store_b64(gm, rws, lo, 0, AUX_SC1);
      float f = __builtin_amdgcn_rcpf(L);
      PIN(f); PIN(o0); PIN(o1);
      STAMP(7);
      wait_ge<0>(acnt, C_K + 64);
#pragma unroll
      for (int t = 0; t < 4; ++t) vf[t] = ldtr2(va[t] + 16384, vb[t] + 16384);
      STAMP(8);
      o0 = mfma(vf[0], pf[1], o0); o1 = mfma(vf[1], pf[3], o1); o0 = mfma(vf[2], pf[5], o0); o1 = mfma(vf[3], pf[7], o1);
      BS128(pack24((o0 + o1) * f, tag), rws, so, AUX_SC1);
      STAMP(9);
    }
    {
      u32x4 of[7], av[4];
      poll_o_start(rws, og);
      wait_ge<0>(acnt, 108);
#pragma unroll
      for (int t = 0; t < 7; ++t) of[t] = ldtr2(wa[t], wb[t]);
#pragma unroll
      for (int t = 0; t < 7; ++t) PIN(of[t]);
      float e0, e1, e2, e3;
      unsigned wt = ld32(wta);
      PIN(wt);
      const unsigned cap = poll_o(rws, og, tt, e0, e1, e2, e3);
      SPINS(SPIN_CAP - cap);
      STAMP(10);
#pragma nounroll
      for (unsigned spin = 0; __any((wt & 1u) == 0) && spin < SPIN_CAP * 8; ++spin) { CFENCE(); wt = ld32(wta); }
      const float wf = __uint_as_float(wt);
      e0 *= wf; e1 *= wf; e2 *= wf; e3 *= wf;
      e0 += dpp<ROR4>(e0); e1 += dpp<ROR4>(e1); e2 += dpp<ROR4>(e2); e3 += dpp<ROR4>(e3);
      e0 += dpp<ROR8>(e0); e1 += dpp<ROR8>(e1); e2 += dpp<ROR8>(e2); e3 += dpp<ROR8>(e3);
      st64(owa, u32x2{cvtpk(swap16_add(e0), swap16_add(e1)), cvtpk(swap16_add(e2), swap16_add(e3))});
      CFENCE();
      st32(fwa + (L_OF - L_QF), 1u);
#pragma nounroll
      for (unsigned spin = 0;; ++spin) {
        u32x4 fl; unsigned fh;
        asm volatile("ds_read_b128 %[fl], %[a] offset:%c[qf]\n\tds_read_b32 %[fh], %[a] offset:%c[hf]\n\t"
                     "ds_read_b128 %[q0], %[a0]\n\tds_read_b128 %[q1], %[a1]\n\tds_read_b128 %[q2], %[a2]\n\tds_read_b128 %[q3], %[a3]\n\ts_waitcnt lgkmcnt(0)"
                     : [fl] "=&v"(fl), [fh] "=&v"(fh), [q0] "=&v"(av[0]), [q1] "=&v"(av[1]), [q2] "=&v"(av[2]), [q3] "=&v"(av[3])
                     : [a] "v"(acnt), [a0] "v"(aa[0]), [a1] "v"(aa[1]), [a2] "v"(aa[2]), [a3] "v"(aa[3]), [qf] "i"(L_OF - L_CNT), [hf] "i"(L_HF - L_CNT) : "memory");
        if (rfl(fl[0] & fl[1] & fl[2] & fl[3] & fh) || spin > SPIN_CAP * 8) break;
      }
      STAMP(11);
      f32x4 x0 = {0.f, 0.f, 0.f, 0.f}, x1 = x0;
#pragma unroll
      for (int t = 0; t < 7; ++t) { if (t & 1) x1 = mfma(av[t >> 1], of[t], x1); else x0 = mfma(av[t >> 1], of[t], x0); }
      const float ra = (x0[0] + x0[1]) + (x0[2] + x0[3]), rb = (x1[0] + x1[1]) + (x1[2] + x1[3]);
      BS128(pack24(f32x4{ra, rb, dpp<0xb1>(ra), dpp<0xb1>(rb)}, tag), rws, oo, AUX_SC1);
    }
  } else if (wave == 5) {
    if (j >= 28 || (EXP & 8)) {
      __builtin_amdgcn_s_barrier();
      st32(L_HF, 1u);
    } else {
      const unsigned idx = g * 28 + j;
      const __amdgpu_buffer_rsrc_t rws = mkrsrc(a.ws, WS_END), rb1 = mkrsrc(a.b1, FF * 2);
      __builtin_amdgcn_s_barrier();
      const u32x2 bb = __builtin_bit_cast(u32x2, __builtin_amdgcn_raw_buffer_load_b64(rb1, idx * 16 + (lane & 1) * 8, 0, 0));
      wait_ge<4>(L_CNT, 20);
      const u32x4 xr = ld128(L_X + lane * 16);
      const unsigned tag = ld32(L_TAG) + 1u;
      const unsigned tt = perm(tag, tag, 0x01000100u);
      float xv[8];
#pragma unroll
      for (int i = 0; i < 4; ++i) { xv[2 * i] = blo(xr[i]); xv[2 * i + 1] = bhi(xr[i]); }
#pragma unroll 1
      for (unsigned r = 0; r < 2; ++r) layernorm8(xv, lane < 56);
      st128(L_XN + 4096 + lane * 16, u32x4{perm(__float_as_uint(xv[1]), __float_as_uint(xv[0]), 0x07060302u), perm(__float_as_uint(xv[3]), __float_as_uint(xv[2]), 0x07060302u),
                                           perm(__float_as_uint(xv[5]), __float_as_uint(xv[4]), 0x07060302u), perm(__float_as_uint(xv[7]), __float_as_uint(xv[6]), 0x07060302u)});
      CFENCE();
      u32x4 yf[8];
      const unsigned ya = L_XN + 4096 + (idx & 7) * 112 + grp * 16;
#pragma unroll
      for (int t = 0; t < 4; ++t) { yf[2 * t] = ld128(n == (unsigned)t ? ya : L_ZERO); yf[2 * t + 1] = ld128(n == (unsigned)t && grp < 3 ? ya + 64 : L_ZERO); }
      unsigned ha[4], hb[4];
#pragma unroll
      for (int t = 0; t < 4; ++t) {
        ha[t] = L_W1 + (8 * grp + q) * 128 + ((t ^ (q >> 1)) << 5) + p * 8; PIN(ha[t]);
        hb[t] = ha[t] + (grp < 3 ? 4096 : 0); PIN(hb[t]);
      }
      unsigned hs = (n < 4 ? 0u : OOB) | (WS_H + idx * 256 + (4 * n + grp) * 16);
      unsigned ho = WS_H + ((idx & ~7u) + (n >> 1)) * 256 + (idx & 7) * 32 + (lane & 1) * 16;
      PIN(hs); PIN(ho);
      if ((WARM & 1) && W1POS) { unsigned d = __builtin_amdgcn_raw_buffer_load_b32(rws, ho, 0, AUX_SC1); PIN(d); }
#pragma unroll
      for (int t = 0; t < 8; ++t) PIN(yf[t]);
      STAMP(1);
      if (W1POS) { wait_ge<4>(L_CNT, 76); __builtin_amdgcn_s_setprio(CPRIO); }
      wait_ge<0>(L_CNT, C_W1);
      STAMP(2);
      f32x4 h0 = {0.f, 0.f, 0.f, 0.f}, h1 = h0;
      u32x4 wf[8];
#pragma unroll
      for (int t = 0; t < 4; ++t) { wf[2 * t] = ldtr2(ha[t], (ha[t] ^ 64) + 512); wf[2 * t + 1] = ldtr2(hb[t], (hb[t] ^ 64) + 512); }
#pragma unroll
      for (int t = 0; t < 4; ++t) { h0 = mfma(wf[2 * t], yf[2 * t], h0); h1 = mfma(wf[2 * t + 1], yf[2 * t + 1], h1); }
      BS128(pack24(h0 + h1, tag), rws, hs, AUX_SC1);
      STAMP(3);
      if (W1POS) __builtin_amdgcn_s_sleep(OSL0); else wait_ge<4>(L_CNT, C_K + HSTART);
      u32x4 hv;
      const unsigned cap = poll_g<OSL>(rws, ho, tt, hv);
      SPINS(SPIN_CAP - cap);
      STAMP(4);
      float hh[4];
#pragma unroll
      for (int k = 0; k < 4; ++k) {
        float s = (k & 1) ? odd24(hv[k - 1], hv[k]) : __uint_as_float(hv[k]);
        s += dpp<ROR2>(s); s += dpp<ROR4>(s); s += dpp<ROR8>(s);
        const float z = rbf(rbf(s) + ((k & 1) ? bhi(bb[k >> 1]) : blo(bb[k >> 1])));
        const float inner = 0.7978845608028654f * (z + 0.044715f * z * z * z);
        const float e = __builtin_amdgcn_exp2f(inner * (2.f * 1.4426950408889634f));
        const float th = 1.f - 2.f * __builtin_amdgcn_rcpf(e + 1.f);
        hh[k] = 0.5f * z * (1.f + th);
      }
      st64(L_OB + (lane & 1) * 16 + 8, u32x2{cvtpk(hh[0], hh[1]), cvtpk(hh[2], hh[3])});
      CFENCE();
      st32(L_HF, 1u);
      STAMP(5);
    }
  } else if (wave == 6) {
    __builtin_amdgcn_s_barrier();
    if (!(EXP & 8)) {
      const __amdgpu_buffer_rsrc_t rws = mkrsrc(a.ws, WS_END);
      const unsigned lg = WS_L + g * 4096 + (LSEP ? (lane & 31) * 128 + (j >> 4) * 8 : (j >> 4) * 256 + (lane & 31) * 8);
      wait_ge<4>(L_CNT, 20);
      const unsigned gam = g * 16 + j - 16;
      const unsigned col = 128 * ((gam >> 3) & 3) + 16 * (gam >> 5) + 2 * (gam & 7) + (grp & 1) + (grp >> 1) * 64;
      unsigned fo = WS_OUT + lane * 2048 + gam * 16;
      float fin = 0.f;
      if (j >= 16) { const unsigned fc = __builtin_amdgcn_raw_buffer_load_b16(mkrsrc(a.b2, D * 2), col * 2, 0, 0); fin = blo(*(LAS u16*)(size_t)(L_X + col * 2)) + blo(fc); }
      PIN(fin); PIN(fo);
      if (WARM & 1) { unsigned d = __builtin_amdgcn_raw_buffer_load_b32(rws, lg, 0, AUX_SC1); PIN(d); }
      wait_ge<2>(L_CNT, C_K + LSTART);
      const unsigned tag = ld32(L_TAG) + 1u;
      __builtin_amdgcn_s_setprio(CPRIO);
      STAMP(1);
      float ls;
      const unsigned capl = poll_l(rws, lg, tag, ls);
      SPINS(SPIN_CAP - capl);
      STAMP(2);
      float m = ls;
      m = fmaxf(m, dpp<ROR1>(m)); m = fmaxf(m, dpp<ROR2>(m)); m = fmaxf(m, dpp<ROR4>(m)); m = fmaxf(m, dpp<ROR8>(m)); m = swap16_max(m);
      const float e = __builtin_amdgcn_exp2f(ls - m);
      float l = e;
      l += dpp<ROR1>(l); l += dpp<ROR2>(l); l += dpp<ROR4>(l); l += dpp<ROR8>(l); l = swap16_add(l);
      st32(L_WT + (lane & 31) * 4, __float_as_uint(e * __builtin_amdgcn_rcpf(l)) | 1u);
      STAMP(3);
      if (j >= 16) {
        __builtin_amdgcn_s_sleep(FSL0);
        float d0, d1, d2, d3;
        const unsigned cap = poll_f(rws, fo, perm(tag, tag, 0x01000100u), d0, d1, d2, d3);
        SPINS(SPIN_CAP - cap);
        STAMP(4);
        const auto sa = __builtin_amdgcn_permlane32_swap(__float_as_uint(d0), __float_as_uint(d1), false, false);
        const auto sb = __builtin_amdgcn_permlane32_swap(__float_as_uint(d2), __float_as_uint(d3), false, false);
        const auto sc = __builtin_amdgcn_permlane16_swap(__float_as_uint(__uint_as_float(sa[0]) + __uint_as_float(sa[1])), __float_as_uint(__uint_as_float(sb[0]) + __uint_as_float(sb[1])), false, false);
        float r = __uint_as_float(sc[0]) + __uint_as_float(sc[1]);
        r += dpp<ROR8>(r); r += dpp<ROR4>(r); r += dpp<ROR2>(r); r += dpp<ROR1>(r);
        r += fin;
        __builtin_amdgcn_raw_buffer_store_b16((u16)cvtpk(r, r), mkrsrc(a.out, D * 2), n == 0 ? col * 2 : OOB, 0, 0);
        if (gam == 0) __builtin_amdgcn_raw_buffer_store_b32(tag, rws, lane == 0 ? WS_EPOCH : OOB, 0, AUX_SC1);
      }
    }
  }
  FINISH();
}

extern "C" void kernel_launch(void* const* d_in, const int* in_sizes, int n_in, void* d_out, int out_size, void* d_ws, size_t ws_size, hipStream_t stream) {
  Args a;
  a.x = (const u16*)d_in[0]; a.kc = (const u16*)d_in[1]; a.vc = (const u16*)d_in[2]; a.wq = (const u16*)d_in[3]; a.wo = (const u16*)d_in[4];
  a.w1 = (const u16*)d_in[6]; a.b1 = (const u16*)d_in[7]; a.w2 = (const u16*)d_in[8]; a.b2 = (const u16*)d_in[9];
  a.out = (u16*)d_out; a.ws = (unsigned char*)d_ws;
  fused_decode<<<NWG, NTHR, 0, stream>>>(a);
}
```

```cpp
#include <hip/hip_runtime.h>
#include <hip/hip_bf16.h>
#include <stdint.h>

typedef unsigned short u16;
typedef unsigned u32x4 __attribute__((ext_vector_type(4)));
typedef unsigned u32x2 __attribute__((ext_vector_type(2)));
typedef float f32x4 __attribute__((ext_vector_type(4)));
typedef short s16x4 __attribute__((ext_vector_type(4)));
typedef __bf16 bf16x2 __attribute__((ext_vector_type(2)));
typedef __bf16 bf16x8 __attribute__((ext_vector_type(8)));
#define LAS __attribute__((address_space(3)))
#define GAS __attribute__((address_space(1)))
#define DI __device__ __forceinline__

constexpr int D = 448, FF = 1792;
constexpr int NWG = 256, NWAVE = 7, NTHR = NWAVE * 64;
constexpr unsigned WS_EPOCH = 1024;
constexpr unsigned WS_Q = 4096;
constexpr unsigned WS_H = WS_Q + 8 * 16384;
constexpr unsigned WS_L = WS_H + 256 * 256;
constexpr unsigned WS_O = WS_L + 8 * 4096;
constexpr unsigned WS_OUT = WS_O + 8 * 65536;
#define FR 0
constexpr unsigned FREP = FR ? 256 * 2048 + 4096 : 0;
#define FSP 0
constexpr unsigned WS_END = WS_OUT + (256 * 2048 << FSP) + 2 * FREP;
constexpr unsigned WS_STAMP = 8u << 20;
constexpr unsigned SPIN_CAP = 1u << 16;
constexpr unsigned OOB = 0x80000000u;
constexpr int AUX_SC1 = 16;
constexpr float CS = 0.0625f * 1.4426950408889634f;
constexpr unsigned L_WQ = 0, L_X = 14336, L_TAG = 15360, L_K = 16384, L_V = 49152, L_W1 = 81920, L_WO = 89088, L_W2 = 103424, L_MISC = 110592;
constexpr unsigned L_CNT = L_MISC;
constexpr unsigned L_Z0 = L_MISC + 256;
constexpr unsigned L_QF = L_Z0, L_OF = L_Z0 + 16, L_HF = L_Z0 + 32, L_RF = L_Z0 + 48;
constexpr unsigned L_M = L_Z0 + 64, L_LS = L_Z0 + 96;
constexpr unsigned L_WT = L_Z0 + 128;
constexpr unsigned L_OB = L_Z0 + 256;
constexpr unsigned L_ZERO = L_Z0 + 512;
constexpr unsigned L_XN = L_Z0 + 1024;
constexpr unsigned L_QB = L_XN + 5120;
constexpr unsigned L_PB = L_QB + 1152;
constexpr unsigned L_RED = L_PB + 384;
constexpr unsigned L_END = L_RED + 128;
static_assert(L_END <= 163840, "lds");
#define PF 1
#define KAL 4096
#define DEFER 1
#define LEARLY 1
#define FT 0
#define FOWN 16
#define PWF 0
#define PWO 0
#define FA 0
#define ASL 0
#define OAUX 0
#define PFN 0
#define PFX ""
#define PFM "0"
#define LFL 16
#define TRIM 1
#define GM 1
#define OPK 0
#define EAUX 16
#define EXP 0
#define WA 20
#define WB 20
#define WC 32
#define QSTART 16
#define LPRIO 1
#define CPRIO 3
#define OSL0 14
#define OSL 5
#define LSTART 72
#define WARM 1
#define LSEP 1
#define QPAD 0
#define QOFF "2048"
#define W1POS 0
#define FSL0 38
#define FSL 5
#define HSTART 44
#define LSL 5
#define NTQ "nt"
#define NTO "nt"
static_assert(WA >= 8 && WA % 4 == 0 && WB % 4 == 0 && WC % 4 == 0 && WA <= 60 && WB <= 60 && WC <= 60, "window");
constexpr int C_K = W1POS ? 16 : 24, C_W1 = W1POS ? 88 : 24;

DI unsigned cvtpk(float lo, float hi) { bf16x2 r; r.x = (__bf16)lo; r.y = (__bf16)hi; return __builtin_bit_cast(unsigned, r); }
DI float rbf(float f) { return __uint_as_float(cvtpk(f, f) & 0xffff0000u); }
DI float blo(unsigned v) { return __uint_as_float(v << 16); }
DI float bhi(unsigned v) { return __uint_as_float(v & 0xffff0000u); }
template <int CTRL> DI float dpp(float x) { return __builtin_bit_cast(float, __builtin_amdgcn_mov_dpp(__builtin_bit_cast(int, x), CTRL, 0xf, 0xf, true)); }
constexpr int ROR1 = 0x121, ROR2 = 0x122, ROR4 = 0x124, ROR8 = 0x128;
DI float swap16_add(float a) { auto s = __builtin_amdgcn_permlane16_swap(__float_as_uint(a), __float_as_uint(a), false, false); return __uint_as_float(s[0]) + __uint_as_float(s[1]); }
DI float swap32_add(float a) { auto s = __builtin_amdgcn_permlane32_swap(__float_as_uint(a), __float_as_uint(a), false, false); return __uint_as_float(s[0]) + __uint_as_float(s[1]); }
DI float swap16_max(float a) { auto s = __builtin_amdgcn_permlane16_swap(__float_as_uint(a), __float_as_uint(a), false, false); return fmaxf(__uint_as_float(s[0]), __uint_as_float(s[1])); }
DI float swap32_max(float a) { auto s = __builtin_amdgcn_permlane32_swap(__float_as_uint(a), __float_as_uint(a), false, false); return fmaxf(__uint_as_float(s[0]), __uint_as_float(s[1])); }
DI float wave_sum(float x) { x += dpp<ROR8>(x); x += dpp<ROR4>(x); x += dpp<ROR2>(x); x += dpp<ROR1>(x); return swap32_add(swap16_add(x)); }
DI __amdgpu_buffer_rsrc_t mkrsrc(const void* p, unsigned bytes) { return __builtin_amdgcn_make_buffer_rsrc(const_cast<void*>(p), 0, bytes, 0x00020000); }
#define BL128(r, off, aux) __builtin_bit_cast(u32x4, __builtin_amdgcn_raw_buffer_load_b128((r), (off), 0, (aux)))
#define BS128(v, r, off, aux) __builtin_amdgcn_raw_buffer_store_b128((v), (r), (off), 0, (aux))
#define CFENCE() asm volatile("" ::: "memory")
#define SB() __builtin_amdgcn_sched_barrier(0)
DI unsigned rfl(unsigned v) { return __builtin_amdgcn_readfirstlane(v); }
DI unsigned ld32(unsigned a) { return *(LAS unsigned*)(size_t)a; }
DI u32x2 ld64(unsigned a) { return *(LAS u32x2*)(size_t)a; }
DI u32x4 ld128(unsigned a) { return *(LAS u32x4*)(size_t)a; }
DI void st32(unsigned a, unsigned v) { *(LAS unsigned*)(size_t)a = v; }
DI void st64(unsigned a, u32x2 v) { *(LAS u32x2*)(size_t)a = v; }
DI void st128(unsigned a, u32x4 v) { *(LAS u32x4*)(size_t)a = v; }
DI u32x2 ldtr(unsigned a) { return __builtin_bit_cast(u32x2, __builtin_amdgcn_ds_read_tr16_b64_v4i16((LAS s16x4*)(size_t)a)); }
DI u32x4 ldtr2(unsigned a, unsigned b) { u32x2 x = ldtr(a), y = ldtr(b); return u32x4{x[0], x[1], y[0], y[1]}; }
DI f32x4 mfma(u32x4 a, u32x4 b, f32x4 c) { return __builtin_amdgcn_mfma_f32_16x16x32_bf16(__builtin_bit_cast(bf16x8, a), __builtin_bit_cast(bf16x8, b), c, 0, 0, 0); }
DI unsigned tagbad(const u32x4& v, unsigned tag) { return (v[1] ^ tag) | (v[3] ^ tag); }

#define STAMP(i) do {} while (0)
#define SPINS(n) do {} while (0)

DI void layernorm8(float (&v)[8], bool valid) {
  const float piv = __builtin_bit_cast(float, __builtin_amdgcn_readfirstlane(__builtin_bit_cast(int, v[0])));
  float s1 = 0.f, s2 = 0.f;
#pragma unroll
  for (int i = 0; i < 8; ++i) { const float d = v[i] - piv; s1 += d; s2 = fmaf(d, d, s2); }
  s1 = valid ? s1 : 0.f; s2 = valid ? s2 : 0.f;
  s1 = wave_sum(s1); s2 = wave_sum(s2);
  const float md = s1 * (1.f / D);
  const float var = fmaxf(s2 * (1.f / D) - md * md, 0.f);
  const float m = rbf(piv + md);
  const float den = rbf(__builtin_amdgcn_sqrtf(rbf(rbf(var) + 1e-5f)));
  const float inv = 1.0f / den;
#pragma unroll
  for (int i = 0; i < 8; ++i) v[i] = rbf(rbf(v[i] - m) * inv);
}

struct Args {
  const u16* x; const u16* wq; const u16* kc; const u16* vc; const u16* w1; const u16* wo; const u16* w2; unsigned char* ws;
  const u16* b1; const u16* b2; u16* out;
};

#define PIN(x) asm volatile("" : "+v"(x))
template <int SLEEP> DI void wait_ge(unsigned a, int need) {
  unsigned t, sv, cap;
  asm volatile("s_mov_b32 %[cap], %[C]\n1:\n\tds_read_b32 %[t], %[a]\n\ts_waitcnt lgkmcnt(0)\n\tv_readfirstlane_b32 %[sv], %[t]\n\ts_cmp_ge_i32 %[sv], %[n]\n\ts_cbranch_scc1 2f\n\t"
               "s_sleep %[sl]\n\ts_sub_u32 %[cap], %[cap], 1\n\ts_cbranch_scc0 1b\n2:"
               : [t] "=&v"(t), [sv] "=&s"(sv), [cap] "=&s"(cap) : [a] "v"(a), [n] "s"(need), [C] "i"(SPIN_CAP * 8), [sl] "i"(SLEEP) : "memory", "scc");
}
DI unsigned perm(unsigned a, unsigned b, unsigned sel) { return __builtin_amdgcn_perm(a, b, sel); }
DI u32x4 pack24(f32x4 v, unsigned tag) {
  const u32x4 ai = __builtin_bit_cast(u32x4, v);
  return u32x4{perm(ai[0], ai[1], 0x07060503u), perm(ai[1], tag, 0x06050100u), perm(ai[2], ai[3], 0x07060503u), perm(ai[3], tag, 0x06050100u)};
}
DI float odd24(unsigned a, unsigned b) { return __uint_as_float(perm(a, b, 0x0403020cu)); }
#define PCLOB "v200", "v201", "v202", "v203", "v204", "v205", "v206", "v207", "v208", "v209", "v210", "v211", "v212", "v213", "v214", "v215"
#define QISS "buffer_load_dwordx4 v[200:203], %[oa], %[r], 0 offen sc1\n\tbuffer_load_dwordx4 v[204:207], %[oa], %[r], 0 offen offset:" QOFF " sc1\n\t"
DI void poll_q_start(__amdgpu_buffer_rsrc_t r, unsigned oa) { asm volatile(QISS :: [r] "s"(r), [oa] "v"(oa) : "memory", PCLOB); }
DI unsigned poll_q(__amdgpu_buffer_rsrc_t r, unsigned oa, unsigned tt, float& d0, float& d1, float& d2, float& d3) {
  unsigned t, u, cap;
  asm volatile("s_mov_b32 %[cap], %[C]\n"
               "1:\n\ts_waitcnt vmcnt(0)\n\tv_perm_b32 %[t], v201, v203, %[sel]\n\tv_perm_b32 %[u], v205, v207, %[sel]\n\tv_xor_b32 %[t], %[t], %[tt]\n\tv_xor_b32 %[u], %[u], %[tt]\n\tv_or_b32 %[t], %[t], %[u]\n\t"
               "v_cmp_ne_u32 vcc, 0, %[t]\n\ts_cbranch_vccz 2f\n\ts_mov_b64 exec, vcc\n\t" QISS
               "s_sub_u32 %[cap], %[cap], 1\n\ts_cbranch_scc0 1b\n\ts_waitcnt vmcnt(0)\n"
               "2:\n\ts_mov_b64 exec, -1\n\tv_add_f32 %[d0], v200, v204\n\tv_add_f32 %[d2], v202, v206\n\t"
               "v_perm_b32 %[t], v200, v201, %[so]\n\tv_perm_b32 %[u], v204, v205, %[so]\n\tv_add_f32 %[d1], %[t], %[u]\n\t"
               "v_perm_b32 %[t], v202, v203, %[so]\n\tv_perm_b32 %[u], v206, v207, %[so]\n\tv_add_f32 %[d3], %[t], %[u]\n\ts_nop 1"
               : [d0] "=&v"(d0), [d1] "=&v"(d1), [d2] "=&v"(d2), [d3] "=&v"(d3), [t] "=&v"(t), [u] "=&v"(u), [cap] "=&s"(cap)
               : [r] "s"(r), [oa] "v"(oa), [tt] "v"(tt), [sel] "s"(0x05040100u), [so] "s"(0x0403020cu), [C] "i"(SPIN_CAP) : "memory", "scc", "vcc", PCLOB);
  return cap;
}
template <int SL> DI unsigned poll_g(__amdgpu_buffer_rsrc_t r, unsigned oa, unsigned tt, u32x4& d) {
  unsigned t, cap;
  asm volatile("s_mov_b32 %[cap], %[C]\n"
               "1:\n\tbuffer_load_dwordx4 v[200:203], %[oa], %[r], 0 offen sc1\n\ts_waitcnt vmcnt(0)\n\tv_perm_b32 %[t], v201, v203, %[sel]\n\t"
               "v_cmp_ne_u32 vcc, %[tt], %[t]\n\ts_cbranch_vccz 2f\n\ts_mov_b64 exec, vcc\n\ts_sleep %[sl]\n\t"
               "s_sub_u32 %[cap], %[cap], 1\n\ts_cbranch_scc0 1b\n"
               "2:\n\ts_mov_b64 exec, -1\n\tv_mov_b32 %[d0], v200\n\tv_mov_b32 %[d1], v201\n\tv_mov_b32 %[d2], v202\n\tv_mov_b32 %[d3], v203"
               : [d0] "=&v"(d[0]), [d1] "=&v"(d[1]), [d2] "=&v"(d[2]), [d3] "=&v"(d[3]), [t] "=&v"(t), [cap] "=&s"(cap)
               : [r] "s"(r), [oa] "v"(oa), [tt] "v"(tt), [sel] "s"(0x05040100u), [C] "i"(SPIN_CAP), [sl] "i"(SL) : "memory", "scc", "vcc", PCLOB);
  return cap;
}

#define OISS(A) "buffer_load_dwordx4 v[" A "], %[oa], %[r], 0 offen sc1\n\t"
#define OCHK(A, r1, r3, L) "s_waitcnt vmcnt(2)\n\tv_perm_b32 %[t], v" r1 ", v" r3 ", %[sel]\n\tv_cmp_ne_u32 vcc, %[tt], %[t]\n\ts_cbranch_vccz " L "f\n\t" OISS(A)
#define OOUT(L, r0, r1, r2, r3) L ":\n\tv_mov_b32 %[d0], v" r0 "\n\tv_perm_b32 %[d1], v" r0 ", v" r1 ", %[so]\n\tv_mov_b32 %[d2], v" r2 "\n\tv_perm_b32 %[d3], v" r2 ", v" r3 ", %[so]\n\t"
DI void poll_o_start(__amdgpu_buffer_rsrc_t r, unsigned oa) {
  asm volatile("s_sleep %[s0]\n\t" OISS("200:203") "s_sleep %[sl]\n\t" OISS("204:207") "s_sleep %[sl]\n\t" OISS("208:211") :: [r] "s"(r), [oa] "v"(oa), [s0] "i"(OSL0), [sl] "i"(OSL) : "memory", PCLOB);
}
DI unsigned poll_o(__amdgpu_buffer_rsrc_t r, unsigned oa, unsigned tt, float& d0, float& d1, float& d2, float& d3) {
  unsigned t, cap;
  asm volatile("s_mov_b32 %[cap], %[C]\n"
               "1:\n\t" OCHK("200:203", "201", "203", "2") OCHK("204:207", "205", "207", "3") OCHK("208:211", "209", "211", "4")
               "s_sub_u32 %[cap], %[cap], 1\n\ts_cbranch_scc0 1b\n"
               OOUT("2", "200", "201", "202", "203") "s_branch 9f\n" OOUT("3", "204", "205", "206", "207") "s_branch 9f\n" OOUT("4", "208", "209", "210", "211") "\n9:"
               : [d0] "=&v"(d0), [d1] "=&v"(d1), [d2] "=&v"(d2), [d3] "=&v"(d3), [t] "=&v"(t), [cap] "=&s"(cap)
               : [r] "s"(r), [oa] "v"(oa), [tt] "v"(tt), [sel] "s"(0x05040100u), [so] "s"(0x0403020cu), [C] "i"(SPIN_CAP) : "memory", "scc", "vcc", PCLOB);
  return cap;
}
#define FCLOB "v200", "v201", "v202", "v203", "v204", "v205", "v206", "v207", "v208", "v209", "v210", "v211", "v212", "v213", "v214", "v215", "v216", "v217", "v218", "v219", "v220", "v221", "v222", "v223", "v224", "v225", "v226", "v227", "v228", "v229", "v230", "v231", "v232", "v233", "v234", "v235", "v236", "v237", "v238", "v239", "v240", "v241", "v242", "v243", "v244", "v245", "v246", "v247"
DI unsigned poll_f(__amdgpu_buffer_rsrc_t r, unsigned oa, unsigned tt, float& d0, float& d1, float& d2, float& d3) {
  unsigned t, u, y, z, cap, bc;
  asm volatile("s_mov_b32 %[cap], %[C]\n\t"

               "buffer_load_dwordx4 v[200:203], %[oa], %[r], 0 offen sc1\n\tbuffer_load_dwordx4 v[204:207], %[oa], %[r], %[k1] offen sc1\n\tbuffer_load_dwordx4 v[208:211], %[oa], %[r], %[k2] offen sc1\n\tbuffer_load_dwordx4 v[212:215], %[oa], %[r], %[k3] offen sc1\n\t" "s_sleep %[sl]\n\t"
               "buffer_load_dwordx4 v[216:219], %[o1], %[r], 0 offen sc1\n\tbuffer_load_dwordx4 v[220:223], %[o1], %[r], %[k1] offen sc1\n\tbuffer_load_dwordx4 v[224:227], %[o1], %[r], %[k2] offen sc1\n\tbuffer_load_dwordx4 v[228:231], %[o1], %[r], %[k3] offen sc1\n\t" "s_sleep %[sl]\n\t"
               "buffer_load_dwordx4 v[232:235], %[o2], %[r], 0 offen sc1\n\tbuffer_load_dwordx4 v[236:239], %[o2], %[r], %[k1] offen sc1\n\tbuffer_load_dwordx4 v[240:243], %[o2], %[r], %[k2] offen sc1\n\tbuffer_load_dwordx4 v[244:247], %[o2], %[r], %[k3] offen sc1\n\t"
               "\n1:\n\t"
               "s_waitcnt vmcnt(8)\n\tv_perm_b32 %[t], v201, v203, %[sel]\n\tv_perm_b32 %[u], v205, v207, %[sel]\n\tv_perm_b32 %[y], v209, v211, %[sel]\n\tv_perm_b32 %[z], v213, v215, %[sel]\n\tv_xor_b32 %[t], %[t], %[tt]\n\tv_xor_b32 %[u], %[u], %[tt]\n\tv_xor_b32 %[y], %[y], %[tt]\n\tv_xor_b32 %[z], %[z], %[tt]\n\tv_or3_b32 %[t], %[t], %[u], %[y]\n\tv_or_b32 %[t], %[t], %[z]\n\tv_cmp_ne_u32 vcc, 0, %[t]\n\tv_perm_b32 v201, v200, v201, %[so]\n\tv_perm_b32 v205, v204, v205, %[so]\n\tv_perm_b32 v209, v208, v209, %[so]\n\tv_perm_b32 v213, v212, v213, %[so]\n\tv_perm_b32 v203, v202, v203, %[so]\n\tv_perm_b32 v207, v206, v207, %[so]\n\tv_perm_b32 v211, v210, v211, %[so]\n\tv_perm_b32 v215, v214, v215, %[so]\n\tv_pk_add_f32 v[200:201], v[200:201], v[204:205]\n\tv_pk_add_f32 v[208:209], v[208:209], v[212:213]\n\tv_pk_add_f32 v[202:203], v[202:203], v[206:207]\n\tv_pk_add_f32 v[210:211], v[210:211], v[214:215]\n\tv_add_f32 %[d0], v200, v208\n\tv_add_f32 %[d1], v201, v209\n\tv_add_f32 %[d2], v202, v210\n\tv_add_f32 %[d3], v203, v211\n\ts_cbranch_vccz 2f\n\ts_mov_b64 exec, vcc\n\tbuffer_load_dwordx4 v[200:203], %[oa], %[r], 0 offen sc1\n\tbuffer_load_dwordx4 v[204:207], %[oa], %[r], %[k1] offen sc1\n\tbuffer_load_dwordx4 v[208:211], %[oa], %[r], %[k2] offen sc1\n\tbuffer_load_dwordx4 v[212:215], %[oa], %[r], %[k3] offen sc1\n\t"
               "s_waitcnt vmcnt(8)\n\tv_perm_b32 %[t], v217, v219, %[sel]\n\tv_perm_b32 %[u], v221, v223, %[sel]\n\tv_perm_b32 %[y], v225, v227, %[sel]\n\tv_perm_b32 %[z], v229, v231, %[sel]\n\tv_xor_b32 %[t], %[t], %[tt]\n\tv_xor_b32 %[u], %[u], %[tt]\n\tv_xor_b32 %[y], %[y], %[tt]\n\tv_xor_b32 %[z], %[z], %[tt]\n\tv_or3_b32 %[t], %[t], %[u], %[y]\n\tv_or_b32 %[t], %[t], %[z]\n\tv_cmp_ne_u32 vcc, 0, %[t]\n\tv_perm_b32 v217, v216, v217, %[so]\n\tv_perm_b32 v221, v220, v221, %[so]\n\tv_perm_b32 v225, v224, v225, %[so]\n\tv_perm_b32 v229, v228, v229, %[so]\n\tv_perm_b32 v219, v218, v219, %[so]\n\tv_perm_b32 v223, v222, v223, %[so]\n\tv_perm_b32 v227, v226, v227, %[so]\n\tv_perm_b32 v231, v230, v231, %[so]\n\tv_pk_add_f32 v[216:217], v[216:217], v[220:221]\n\tv_pk_add_f32 v[224:225], v[224:225], v[228:229]\n\tv_pk_add_f32 v[218:219], v[218:219], v[222:223]\n\tv_pk_add_f32 v[226:227], v[226:227], v[230:231]\n\tv_add_f32 %[d0], v216, v224\n\tv_add_f32 %[d1], v217, v225\n\tv_add_f32 %[d2], v218, v226\n\tv_add_f32 %[d3], v219, v227\n\ts_cbranch_vccz 2f\n\ts_mov_b64 exec, vcc\n\tbuffer_load_dwordx4 v[216:219], %[o1], %[r], 0 offen sc1\n\tbuffer_load_dwordx4 v[220:223], %[o1], %[r], %[k1] offen sc1\n\tbuffer_load_dwordx4 v[224:227], %[o1], %[r], %[k2] offen sc1\n\tbuffer_load_dwordx4 v[228:231], %[o1], %[r], %[k3] offen sc1\n\t"
               "s_waitcnt vmcnt(8)\n\tv_perm_b32 %[t], v233, v235, %[sel]\n\tv_perm_b32 %[u], v237, v239, %[sel]\n\tv_perm_b32 %[y], v241, v243, %[sel]\n\tv_perm_b32 %[z], v245, v247, %[sel]\n\tv_xor_b32 %[t], %[t], %[tt]\n\tv_xor_b32 %[u], %[u], %[tt]\n\tv_xor_b32 %[y], %[y], %[tt]\n\tv_xor_b32 %[z], %[z], %[tt]\n\tv_or3_b32 %[t], %[t], %[u], %[y]\n\tv_or_b32 %[t], %[t], %[z]\n\tv_cmp_ne_u32 vcc, 0, %[t]\n\tv_perm_b32 v233, v232, v233, %[so]\n\tv_perm_b32 v237, v236, v237, %[so]\n\tv_perm_b32 v241, v240, v241, %[so]\n\tv_perm_b32 v245, v244, v245, %[so]\n\tv_perm_b32 v235, v234, v235, %[so]\n\tv_perm_b32 v239, v238, v239, %[so]\n\tv_perm_b32 v243, v242, v243, %[so]\n\tv_perm_b32 v247, v246, v247, %[so]\n\tv_pk_add_f32 v[232:233], v[232:233], v[236:237]\n\tv_pk_add_f32 v[240:241], v[240:241], v[244:245]\n\tv_pk_add_f32 v[234:235], v[234:235], v[238:239]\n\tv_pk_add_f32 v[242:243], v[242:243], v[246:247]\n\tv_add_f32 %[d0], v232, v240\n\tv_add_f32 %[d1], v233, v241\n\tv_add_f32 %[d2], v234, v242\n\tv_add_f32 %[d3], v235, v243\n\ts_cbranch_vccz 2f\n\ts_mov_b64 exec, vcc\n\tbuffer_load_dwordx4 v[232:235], %[o2], %[r], 0 offen sc1\n\tbuffer_load_dwordx4 v[236:239], %[o2], %[r], %[k1] offen sc1\n\tbuffer_load_dwordx4 v[240:243], %[o2], %[r], %[k2] offen sc1\n\tbuffer_load_dwordx4 v[244:247], %[o2], %[r], %[k3] offen sc1\n\t"
               "s_sub_u32 %[cap], %[cap], 1\n\ts_cbranch_scc0 1b\n"
               "2:\n\ts_mov_b64 exec, -1\n\ts_nop 1"
               : [d0] "=&v"(d0), [d1] "=&v"(d1), [d2] "=&v"(d2), [d3] "=&v"(d3), [t] "=&v"(t), [u] "=&v"(u), [y] "=&v"(y), [z] "=&v"(z), [cap] "=&s"(cap), [bc] "=&s"(bc)
               : [r] "s"(r), [oa] "v"(oa), [o1] "v"(oa + FREP), [o2] "v"(oa + 2 * FREP), [tt] "v"(tt), [sel] "s"(0x05040100u), [so] "s"(0x0403020cu), [C] "i"(SPIN_CAP), [sl] "i"(FSL), [fa] "i"(FA), [asl] "i"(ASL), [k1] "s"(131072u << FSP), [k2] "s"(262144u << FSP), [k3] "s"(393216u << FSP) : "memory", "scc", "vcc", FCLOB);
  return cap;
}
#define LISS(A) "buffer_load_dwordx2 v[" A "], %[oa], %[r], 0 offen sc1\n\t"
#define LCHK(A, r0, r1) "s_waitcnt vmcnt(2)\n\tv_cmp_ne_u32 vcc, %[tg], v" r1 "\n\tv_mov_b32 %[d], v" r0 "\n\ts_cbranch_vccz 2f\n\t" LISS(A)
DI unsigned poll_l(__amdgpu_buffer_rsrc_t r, unsigned oa, unsigned tg, float& d) {
  unsigned cap;
  asm volatile("s_mov_b32 %[cap], %[C]\n\t" LISS("248:249") "s_sleep %[sl]\n\t" LISS("250:251") "s_sleep %[sl]\n\t" LISS("252:253")
               "\n1:\n\t" LCHK("248:249", "248", "249") LCHK("250:251", "250", "251") LCHK("252:253", "252", "253")
               "s_sub_u32 %[cap], %[cap], 1\n\ts_cbranch_scc0 1b\n2:\n\ts_nop 1"
               : [d] "=&v"(d), [cap] "=&s"(cap) : [r] "s"(r), [oa] "v"(oa), [tg] "v"(tg), [C] "i"(SPIN_CAP), [sl] "i"(LSL) : "memory", "scc", "vcc", "v248", "v249", "v250", "v251", "v252", "v253");
  return cap;
}

#define FINISH() do {} while (0)

__global__ void __attribute__((aligned(KAL))) __launch_bounds__(NTHR, 2) fused_decode(Args a_) {
  __shared__ __attribute__((aligned(16))) unsigned char smem[L_END];
  const __attribute__((address_space(4))) Args* ap = (const __attribute__((address_space(4))) Args*)__builtin_amdgcn_kernarg_segment_ptr();
  const unsigned lane = threadIdx.x & 63, wave = rfl(threadIdx.x >> 6);
  const unsigned b = blockIdx.x, g = b & 7, j = b >> 3;
  asm volatile("" :: "s"((unsigned)(size_t)(LAS unsigned char*)smem));

#define PFC "s_getpc_b64 %[pc]\n1:\n\tv_lshlrev_b32 %[z], 5, %[t]\n\tv_and_b32 %[z], 0x3f80, %[z]\n\tv_min_u32 %[z], fd_end5 - 1b, %[z]\n\tglobal_load_dword v200, %[z], %[pc] " PFX "\n\t"
#define PFA PFC
#define PFB
  { unsigned long long pc_; unsigned t0_, t1_;
    asm volatile("s_cmp_lg_u32 %[w], 0\n\ts_cbranch_scc1 2f\n\ts_load_dword s96, %[ka], 0\n2:\n\t" PFA "s_cbranch_scc0 3f\n\t" PFB
                 "v_lshlrev_b32 %[a], 2, %[t]\n\tv_add_u32 %[a], %[lc], %[a]\n\tv_mov_b32 %[z], 0\n\tds_write_b32 %[a], %[z]\n\ts_waitcnt lgkmcnt(0)\n3:\n\ts_barrier"
                 : [pc] "=&s"(pc_), [a] "=&v"(t0_), [z] "=&v"(t1_), [ka] "+s"(ap)
                 : [t] "v"(threadIdx.x), [lc] "i"(L_CNT - 256), [w] "s"(wave), [sl] "s"((j * 7 + wave) << (PF >= 5)) : "memory", "v200", "s96", "scc"); }
#define MEET() do {} while (0)
#define LEAVE(id) do { FINISH(); asm volatile("fd_end" #id ":\n\ts_endpgm" ::: "memory"); __builtin_unreachable(); } while (0)
#define RARE(c) __builtin_expect_with_probability((c), 0, 0.9999)
  unsigned w5 = wave, w6 = wave;
  const unsigned grp = lane >> 4, n = lane & 15, q = n >> 2, p = lane & 3;
  if (RARE(wave - 1 < 4)) {
    MEET();
    const unsigned w = wave - 1;
    __builtin_amdgcn_s_setprio(CPRIO);
    const __amdgpu_buffer_rsrc_t rws = mkrsrc(ap->ws, WS_END);
    const unsigned krow = 16 * (grp >> 1) + 4 * (grp & 1) + q;
    unsigned acnt = L_CNT;
    unsigned xl = L_X + lane * 16;
    unsigned xa = L_XN + w * 1024 + (lane >> 2) * 64 + (4 * ((lane >> 1) & 1) + (lane & 1)) * 8;
    unsigned xra = L_XN + w * 1024 + (j & 3) * 224 + grp * 16, xrb = grp < 2 ? xra + 192 : L_ZERO;
    unsigned pa = L_WQ + krow * 128 + ((w ^ ((krow >> 1) & 3)) << 5) + p * 8, pb = (pa & ~2048u) + 12288;
    unsigned po = (n == 0 ? 0u : OOB) | (QPAD ? WS_Q + g * 16384 + (j & 2) * 4096 + (j >> 3) * 2048 + (j & 1) * 1024 + ((j >> 2) & 1) * 512 + w * 128 + grp * 16 : WS_Q + g * 16384 + (j & 3) * 2048 + ((j >> 2) * 16 + 4 * w + grp) * 16);
    PIN(acnt); PIN(xl); PIN(xa); PIN(xra); PIN(xrb); PIN(pa); PIN(pb); PIN(po);
    unsigned tag;
    {
      wait_ge<0>(acnt, 2);
      const u32x4 xr = ld128(xl);
      tag = ld32(xl + 1024 - lane * 16) + 1u;
      float xv[8];
#pragma unroll
      for (int i = 0; i < 4; ++i) { xv[2 * i] = blo(xr[i]); xv[2 * i + 1] = bhi(xr[i]); }
      layernorm8(xv, lane < 56);
      st64(xa, u32x2{perm(__float_as_uint(xv[1]), __float_as_uint(xv[0]), 0x07060302u), perm(__float_as_uint(xv[3]), __float_as_uint(xv[2]), 0x07060302u)});
      st64(xa + 16, u32x2{perm(__float_as_uint(xv[5]), __float_as_uint(xv[4]), 0x07060302u), perm(__float_as_uint(xv[7]), __float_as_uint(xv[6]), 0x07060302u)});
      CFENCE();
      u32x4 xf[4];
#pragma unroll
      for (int ks = 0; ks < 3; ++ks) xf[ks] = ld128(xra + ks * 64);
      xf[3] = ld128(xrb);
#pragma unroll
      for (int ks = 0; ks < 4; ++ks) PIN(xf[ks]);
      PIN(tag);
      STAMP(1);
      wait_ge<0>(acnt, 12);
      f32x4 acc = {0.f, 0.f, 0.f, 0.f}, acc2 = acc;
      { const u32x4 w0 = ldtr2(pa, pa + 1024), w1 = ldtr2(pa + 4096, pa + 5120);
        acc = mfma(w0, xf[0], acc); acc2 = mfma(w1, xf[1], acc2); }
      PIN(acc); PIN(acc2);
      u32x2 wl0, wl1, wl2, wl3;
      { unsigned t, sv, cap;
        asm volatile("s_mov_b32 %[cap], %[C]\n1:\n\tds_read_b32 %[t], %[a]\n\tds_read_b64_tr_b16 %[w0], %[pa] offset:8192\n\tds_read_b64_tr_b16 %[w1], %[pa] offset:9216\n\t"
                     "ds_read_b64_tr_b16 %[w2], %[pb]\n\tds_read_b64_tr_b16 %[w3], %[pb] offset:1024\n\ts_waitcnt lgkmcnt(4)\n\t"
                     "v_readfirstlane_b32 %[sv], %[t]\n\ts_cmp_ge_i32 %[sv], 16\n\ts_cbranch_scc1 2f\n\ts_sub_u32 %[cap], %[cap], 1\n\ts_cbranch_scc0 1b\n2:\n\ts_waitcnt lgkmcnt(0)"
                     : [t] "=&v"(t), [sv] "=&s"(sv), [cap] "=&s"(cap), [w0] "=&v"(wl0), [w1] "=&v"(wl1), [w2] "=&v"(wl2), [w3] "=&v"(wl3) : [a] "v"(acnt), [pa] "v"(pa), [pb] "v"(pb), [C] "i"(SPIN_CAP * 8) : "memory", "scc"); }
      STAMP(2);
      acc = mfma(u32x4{wl0[0], wl0[1], wl1[0], wl1[1]}, xf[2], acc); acc2 = mfma(u32x4{wl2[0], wl2[1], wl3[0], wl3[1]}, xf[3], acc2);
      BS128(pack24(acc + acc2, tag), rws, po, AUX_SC1);
      STAMP(3);
    }
    SB();
    unsigned lane_ = lane; PIN(lane_);
    const unsigned grp_ = lane_ >> 4, n_ = lane_ & 15, q_ = n_ >> 2, p_ = lane_ & 3;
    unsigned qo = QPAD ? WS_Q + g * 16384 + (lane_ >> 5) * 8192 + w * 2048 + ((lane_ >> 2) & 7) * 128 + p_ * 16 : WS_Q + g * 16384 + (lane_ >> 5) * 4096 + w * 512 + (lane_ & 31) * 16;
    unsigned qwa = L_QB + (w >> 1) * 576 + (w & 1) * 256 + (lane_ & 31) * 8, fwa = L_QF + w * 4;
    unsigned ka[8];
#pragma unroll
    for (int ks = 0; ks < 8; ++ks) { ka[ks] = L_K + (16 * w + n_) * 512 + (((4 * ks + grp_) ^ n_) << 4); PIN(ka[ks]); }
    unsigned qra = L_QB + (n_ & 1) * 576 + grp_ * 16;
    unsigned mwa = L_M + (n_ & 1) * 16 + w * 4, mra = L_M + (n_ & 1) * 16;
    unsigned pwa = L_PB + (n_ & 1) * 192 + (w >> 1) * 64 + (2 * (w & 1) + (grp_ >> 1)) * 16 + (grp_ & 1) * 4;
    unsigned pr[4], va[4], vb[4];
    const unsigned vr = 8 * (grp_ & 1) + 2 * q_;
#pragma unroll
    for (int t = 0; t < 4; ++t) {
      pr[t] = ((n_ >> 1) & 3) == (unsigned)t ? L_PB + (n_ & 1) * 192 + grp_ * 16 : L_ZERO; PIN(pr[t]);
      const unsigned c16 = 8 * w + 2 * t + (p_ >> 1);
      va[t] = L_V + (16 * (grp_ >> 1) + vr) * 512 + ((c16 ^ vr) << 4) + (p_ & 1) * 8; PIN(va[t]);
      vb[t] = (va[t] ^ 16) + 512; PIN(vb[t]);
    }
    unsigned lo = ((w == 0 && lane_ < LFL) ? 0u : OOB) | (WS_L + g * 4096 + (LSEP ? j * 128 + lane_ * 8 : lane_ * 256 + j * 8));
    unsigned so = (n_ < 8 ? 0u : OOB) | (WS_O + g * 65536 + j * 2048 + w * 512 + (n_ & 1) * 256 + (n_ >> 1) * 64 + grp_ * 16);
    unsigned og = WS_O + g * 65536 + (8 * w + ((lane_ >> 2) & 7)) * 2048 + ((j >> 2) & 3) * 512 + (j >> 4) * 256 + (j & 3) * 64 + p_ * 16;
    unsigned wta = L_WT + (8 * w + ((lane_ >> 2) & 7)) * 4;
    unsigned owa = L_OB + w * 64 + p_ * 16;
    unsigned wa[7], wb[7];
#pragma unroll
    for (int t = 0; t < 7; ++t) {
      unsigned o = (4 * grp_ + q_) * 896 + w * 32 + p_ * 8 + t * 128; o ^= ((o >> 8) & 3) << 5; wa[t] = o + L_WO; PIN(wa[t]);
      wb[t] = (grp_ < 2 && j < 28) ? wa[t] + 14336 : wa[t]; PIN(wb[t]);
    }
    unsigned aa[4];
#pragma unroll
    for (int v = 0; v < 4; ++v) { aa[v] = q_ == (unsigned)v ? L_OB + (n_ & 3) * 64 + grp_ * 16 : L_ZERO; PIN(aa[v]); }
    unsigned oo = ((lane_ & 1) ? OOB : 0u) | (WS_OUT + ((b * 2048 + w * 512 + lane_ * 8) << FSP));
    PIN(qo); PIN(qwa); PIN(fwa); PIN(qra); PIN(mwa); PIN(mra); PIN(pwa); PIN(lo); PIN(so); PIN(og); PIN(wta); PIN(owa); PIN(oo);
    SB();
    if (PWF) BS128((u32x4{0u, 1u, 0u, 2u}), rws, oo, AUX_SC1);
    if (PWO) BS128((u32x4{0u, 1u, 0u, 2u}), rws, so, AUX_SC1);
    const unsigned tt = perm(tag, tag, 0x01000100u);
    if (EXP & 8) asm volatile("s_endpgm");
    if (WARM & 1) asm volatile("buffer_load_dword v200, %0, %1, 0 offen sc1" :: "v"(og), "s"(rws) : "memory", PCLOB);
    u32x4 kf[8];
    {
      wait_ge<1>(acnt, C_K + QSTART);
      poll_q_start(rws, qo);
      wait_ge<0>(acnt, C_K + 8 + 8 * w);
#pragma unroll
      for (int ks = 0; ks < 8; ++ks) kf[ks] = ld128(ka[ks]);
#pragma unroll
      for (int ks = 0; ks < 8; ++ks) PIN(kf[ks]);
      float d0, d1, d2, d3;
      const unsigned cap = poll_q(rws, qo, tt, d0, d1, d2, d3);
      SPINS(SPIN_CAP - cap);
      st64(qwa, u32x2{cvtpk(swap32_add(d0), swap32_add(d1)), cvtpk(swap32_add(d2), swap32_add(d3))});
      CFENCE();
      st32(fwa, 1u);
      STAMP(4);
    }
    float M;
    {
      u32x4 qf[8];
#pragma nounroll
      for (unsigned spin = 0;; ++spin) {
        u32x4 fl;
        asm volatile("ds_read_b128 %[fl], %[a] offset:%c[qf]\n\t"
                     "ds_read_b128 %[q0], %[qa]\n\tds_read_b128 %[q1], %[qa] offset:64\n\tds_read_b128 %[q2], %[qa] offset:128\n\tds_read_b128 %[q3], %[qa] offset:192\n\t"
                     "ds_read_b128 %[q4], %[qa] offset:256\n\tds_read_b128 %[q5], %[qa] offset:320\n\tds_read_b128 %[q6], %[qa] offset:384\n\tds_read_b128 %[q7], %[qa] offset:448\n\ts_waitcnt lgkmcnt(0)"
                     : [fl] "=&v"(fl), [q0] "=&v"(qf[0]), [q1] "=&v"(qf[1]), [q2] "=&v"(qf[2]), [q3] "=&v"(qf[3]), [q4] "=&v"(qf[4]), [q5] "=&v"(qf[5]), [q6] "=&v"(qf[6]), [q7] "=&v"(qf[7])
                     : [a] "v"(acnt), [qa] "v"(qra), [qf] "i"(L_QF - L_CNT) : "memory");
        if (rfl(fl[0] & fl[1] & fl[2] & fl[3]) || spin > SPIN_CAP * 8) break;
        __builtin_amdgcn_s_sleep(1);
      }
      STAMP(5);
      f32x4 s0 = {0.f, 0.f, 0.f, 0.f}, s1 = s0;
#pragma unroll
      for (int ks = 0; ks < 4; ++ks) { s0 = mfma(kf[2 * ks], qf[2 * ks], s0); s1 = mfma(kf[2 * ks + 1], qf[2 * ks + 1], s1); }
      s0 += s1;
      const unsigned c0 = cvtpk(s0[0], s0[1]), c1 = cvtpk(s0[2], s0[3]);
      const float r0 = blo(c0) * CS, r1 = bhi(c0) * CS, r2 = blo(c1) * CS, r3 = bhi(c1) * CS;
      const float m = swap32_max(swap16_max(fmaxf(fmaxf(r0, r1), fmaxf(r2, r3))));
      st32(mwa, __float_as_uint(m) | 1u);
      u32x4 ma;
#pragma nounroll
      for (unsigned spin = 0;; ++spin) {
        asm volatile("ds_read_b128 %[ma], %[mr]\n\ts_waitcnt lgkmcnt(0)" : [ma] "=&v"(ma) : [mr] "v"(mra) : "memory");
        if (!__any((ma[0] & ma[1] & ma[2] & ma[3] & 1u) == 0) || spin > SPIN_CAP * 8) break;
      }
      M = fmaxf(fmaxf(__uint_as_float(ma[0]), __uint_as_float(ma[1])), fmaxf(__uint_as_float(ma[2]), __uint_as_float(ma[3])));
      const float p0 = __builtin_amdgcn_exp2f(r0 - M), p1 = __builtin_amdgcn_exp2f(r1 - M), p2 = __builtin_amdgcn_exp2f(r2 - M), p3 = __builtin_amdgcn_exp2f(r3 - M);
      st32(pwa, cvtpk(p0, p2)); st32(pwa + 8, cvtpk(p1, p3));
      CFENCE();
      const float l = swap32_add(swap16_add((p0 + p1) + (p2 + p3)));
      st32(mwa + (L_LS - L_M), __float_as_uint(l) | 1u);
      STAMP(6);
    }
    {
      u32x4 pf[8], vf[4], la;
#pragma nounroll
      for (unsigned spin = 0;; ++spin) {
        asm volatile("ds_read_b128 %[fl], %[a] offset:%c[qf]\n\t"
                     "ds_read_b128 %[q0], %[a0]\n\tds_read_b128 %[q1], %[a0] offset:64\n\tds_read_b128 %[q2], %[a1]\n\tds_read_b128 %[q3], %[a1] offset:64\n\t"
                     "ds_read_b128 %[q4], %[a2]\n\tds_read_b128 %[q5], %[a2] offset:64\n\tds_read_b128 %[q6], %[a3]\n\tds_read_b128 %[q7], %[a3] offset:64\n\ts_waitcnt lgkmcnt(0)"
                     : [fl] "=&v"(la), [q0] "=&v"(pf[0]), [q1] "=&v"(pf[1]), [q2] "=&v"(pf[2]), [q3] "=&v"(pf[3]), [q4] "=&v"(pf[4]), [q5] "=&v"(pf[5]), [q6] "=&v"(pf[6]), [q7] "=&v"(pf[7])
                     : [a] "v"(mra), [a0] "v"(pr[0]), [a1] "v"(pr[1]), [a2] "v"(pr[2]), [a3] "v"(pr[3]), [qf] "i"(L_LS - L_M) : "memory");
        if (!__any((la[0] & la[1] & la[2] & la[3] & 1u) == 0) || spin > SPIN_CAP * 8) break;
      }
      f32x4 o0 = {0.f, 0.f, 0.f, 0.f}, o1 = o0;
      const float L = (__uint_as_float(la[0]) + __uint_as_float(la[1])) + (__uint_as_float(la[2]) + __uint_as_float(la[3]));
      const u32x2 gm = {__float_as_uint(M + __builtin_amdgcn_logf(L)), tag};
      if (w == 0) __builtin_amdgcn_raw_buffer_store_b64(gm, rws, lo, 0, AUX_SC1);
      float f = __builtin_amdgcn_rcpf(L);
      PIN(f);
      wait_ge<0>(acnt, C_K + 48);
#pragma unroll
      for (int t = 0; t < 4; ++t) vf[t] = ldtr2(va[t], vb[t]);
      o0 = mfma(vf[0], pf[0], o0); o1 = mfma(vf[1], pf[2], o1); o0 = mfma(vf[2], pf[4], o0); o1 = mfma(vf[3], pf[6], o1);
      PIN(f); PIN(o0); PIN(o1);
      STAMP(7);
      wait_ge<0>(acnt, C_K + 64);
#pragma unroll
      for (int t = 0; t < 4; ++t) vf[t] = ldtr2(va[t] + 16384, vb[t] + 16384);
      STAMP(8);
      o0 = mfma(vf[0], pf[1], o0); o1 = mfma(vf[1], pf[3], o1); o0 = mfma(vf[2], pf[5], o0); o1 = mfma(vf[3], pf[7], o1);
      BS128(pack24((o0 + o1) * f, tag), rws, so, AUX_SC1);
      STAMP(9);
    }
    {
      u32x4 of[7], av[4];
      poll_o_start(rws, og);
      wait_ge<0>(acnt, 108);
#pragma unroll
      for (int t = 0; t < 7; ++t) of[t] = ldtr2(wa[t], wb[t]);
#pragma unroll
      for (int t = 0; t < 7; ++t) PIN(of[t]);
      float e0, e1, e2, e3;
      unsigned wt = ld32(wta);
      PIN(wt);
      const unsigned cap = poll_o(rws, og, tt, e0, e1, e2, e3);
      SPINS(SPIN_CAP - cap);
      STAMP(10);
#pragma nounroll
      for (unsigned spin = 0; __any((wt & 1u) == 0) && spin < SPIN_CAP * 8; ++spin) { CFENCE(); wt = ld32(wta); }
      const float wf = __uint_as_float(wt);
      e0 *= wf; e1 *= wf; e2 *= wf; e3 *= wf;
      e0 += dpp<ROR4>(e0); e1 += dpp<ROR4>(e1); e2 += dpp<ROR4>(e2); e3 += dpp<ROR4>(e3);
      e0 += dpp<ROR8>(e0); e1 += dpp<ROR8>(e1); e2 += dpp<ROR8>(e2); e3 += dpp<ROR8>(e3);
      st64(owa, u32x2{cvtpk(swap16_add(e0), swap16_add(e1)), cvtpk(swap16_add(e2), swap16_add(e3))});
      CFENCE();
      st32(fwa + (L_OF - L_QF), 1u);
#pragma nounroll
      for (unsigned spin = 0;; ++spin) {
        u32x4 fl; unsigned fh;
        asm volatile("ds_read_b128 %[fl], %[a] offset:%c[qf]\n\tds_read_b32 %[fh], %[a] offset:%c[hf]\n\t"
                     "ds_read_b128 %[q0], %[a0]\n\tds_read_b128 %[q1], %[a1]\n\tds_read_b128 %[q2], %[a2]\n\tds_read_b128 %[q3], %[a3]\n\ts_waitcnt lgkmcnt(0)"
                     : [fl] "=&v"(fl), [fh] "=&v"(fh), [q0] "=&v"(av[0]), [q1] "=&v"(av[1]), [q2] "=&v"(av[2]), [q3] "=&v"(av[3])
                     : [a] "v"(acnt), [a0] "v"(aa[0]), [a1] "v"(aa[1]), [a2] "v"(aa[2]), [a3] "v"(aa[3]), [qf] "i"(L_OF - L_CNT), [hf] "i"(L_HF - L_CNT) : "memory");
        if (rfl(fl[0] & fl[1] & fl[2] & fl[3] & fh) || spin > SPIN_CAP * 8) break;
      }
      STAMP(11);
      st32(acnt + (L_RF - L_CNT), 1u);
      f32x4 x0 = {0.f, 0.f, 0.f, 0.f}, x1 = x0;
#pragma unroll
      for (int t = 0; t < 7; ++t) { if (t & 1) x1 = mfma(av[t >> 1], of[t], x1); else x0 = mfma(av[t >> 1], of[t], x0); }
      const float ra = (x0[0] + x0[1]) + (x0[2] + x0[3]), rb = (x1[0] + x1[1]) + (x1[2] + x1[3]);
      const u32x4 og4 = pack24(f32x4{ra, rb, dpp<0xb1>(ra), dpp<0xb1>(rb)}, tag);
      if (FR == 2) { __builtin_amdgcn_raw_buffer_store_b128(og4, rws, oo, 2 * FREP, AUX_SC1); __builtin_amdgcn_raw_buffer_store_b128(og4, rws, oo, FREP, AUX_SC1); }
      BS128(og4, rws, oo, AUX_SC1);
      if (FR == 1) { __builtin_amdgcn_raw_buffer_store_b128(og4, rws, oo, FREP, AUX_SC1); __builtin_amdgcn_raw_buffer_store_b128(og4, rws, oo, 2 * FREP, AUX_SC1); }
    }
    LEAVE(1);
  }
  asm volatile("" : "+s"(w6));
  if (RARE(w6 == 6)) {
    MEET();
    if (!(EXP & 8)) {
      const __amdgpu_buffer_rsrc_t rws = mkrsrc(ap->ws, WS_END);
      const unsigned lg = WS_L + g * 4096 + (LSEP ? (lane & 31) * 128 + (j >> 4) * 8 : (j >> 4) * 256 + (lane & 31) * 8);
      wait_ge<4>(L_CNT, 20);
      const unsigned gam = GM == 0 ? g * 16 + j - 16 : GM == 1 ? ((g & 1) * 2 + ((j >> 3) & 1)) * 32 + (g >> 1) * 8 + (j & 7) : (j >> 3) * 32 + (g >> 1) * 8 + (j & 7);
      const bool fc_on = GM == 2 ? (g & 1) == 0 : j >= 16;
      const unsigned col = 128 * ((gam >> 3) & 3) + 16 * (gam >> 5) + 2 * (gam & 7) + (grp & 1) + (grp >> 1) * 64;
      unsigned fo = WS_OUT + ((lane * 2048 + gam * 16) << FSP);
      float fin = 0.f;
      if (fc_on) { const unsigned fc = __builtin_amdgcn_raw_buffer_load_b16(mkrsrc(ap->b2, D * 2), col * 2, 0, 0); fin = blo(*(LAS u16*)(size_t)(L_X + col * 2)) + blo(fc); }
      PIN(fin); PIN(fo);
      if (WARM & 1) { unsigned d = __builtin_amdgcn_raw_buffer_load_b32(rws, lg, 0, AUX_SC1); PIN(d); }
      wait_ge<2>(L_CNT, C_K + LSTART);
      const unsigned tag = ld32(L_TAG) + 1u;
      __builtin_amdgcn_s_setprio(CPRIO);
      STAMP(1);
      float ls;
      const unsigned capl = poll_l(rws, lg, tag, ls);
      SPINS(SPIN_CAP - capl);
      STAMP(2);
      float m = ls;
      m = fmaxf(m, dpp<ROR1>(m)); m = fmaxf(m, dpp<ROR2>(m)); m = fmaxf(m, dpp<ROR4>(m)); m = fmaxf(m, dpp<ROR8>(m)); m = swap16_max(m);
      const float e = __builtin_amdgcn_exp2f(ls - m);
      float l = e;
      l += dpp<ROR1>(l); l += dpp<ROR2>(l); l += dpp<ROR4>(l); l += dpp<ROR8>(l); l = swap16_add(l);
      st32(L_WT + (lane & 31) * 4, __float_as_uint(e * __builtin_amdgcn_rcpf(l)) | 1u);
      STAMP(3);
      if (fc_on) {
        wait_ge<1>(L_RF, 1);
        __builtin_amdgcn_s_sleep(FOWN - 1);
        float d0, d1, d2, d3;
        const unsigned cap = poll_f(rws, fo, perm(tag, tag, 0x01000100u), d0, d1, d2, d3);
        SPINS(SPIN_CAP - cap);
        STAMP(4);
        const auto sa = __builtin_amdgcn_permlane32_swap(__float_as_uint(d0), __float_as_uint(d1), false, false);
        const auto sb = __builtin_amdgcn_permlane32_swap(__float_as_uint(d2), __float_as_uint(d3), false, false);
        const auto sc = __builtin_amdgcn_permlane16_swap(__float_as_uint(__uint_as_float(sa[0]) + __uint_as_float(sa[1])), __float_as_uint(__uint_as_float(sb[0]) + __uint_as_float(sb[1])), false, false);
        float r = __uint_as_float(sc[0]) + __uint_as_float(sc[1]);
        r += dpp<ROR8>(r); r += dpp<ROR4>(r); r += dpp<ROR2>(r); r += dpp<ROR1>(r);
        r += fin;
        __builtin_amdgcn_raw_buffer_store_b16((u16)cvtpk(r, r), mkrsrc(ap->out, D * 2), n == 0 ? col * 2 : OOB, 0, OAUX);
        if (gam == 0) __builtin_amdgcn_raw_buffer_store_b32(tag, rws, lane == 0 ? WS_EPOCH : OOB, 0, EAUX);
      }
    }
    LEAVE(6);
  }
  asm volatile("" : "+s"(w5));
  if (RARE(w5 == 5)) {
    MEET();
    if (j >= 28 || (EXP & 8)) {
      st32(L_HF, 1u);
    } else {
      const unsigned idx = g * 28 + j;
      const __amdgpu_buffer_rsrc_t rws = mkrsrc(ap->ws, WS_END), rb1 = mkrsrc(ap->b1, FF * 2);
      const u32x2 bb = __builtin_bit_cast(u32x2, __builtin_amdgcn_raw_buffer_load_b64(rb1, idx * 16 + (lane & 1) * 8, 0, 0));
      wait_ge<4>(L_CNT, 20);
      const u32x4 xr = ld128(L_X + lane * 16);
      const unsigned tag = ld32(L_TAG) + 1u;
      const unsigned tt = perm(tag, tag, 0x01000100u);
      float xv[8];
#pragma unroll
      for (int i = 0; i < 4; ++i) { xv[2 * i] = blo(xr[i]); xv[2 * i + 1] = bhi(xr[i]); }
#pragma unroll 1
      for (unsigned r = 0; r < 2; ++r) layernorm8(xv, lane < 56);
      st128(L_XN + 4096 + lane * 16, u32x4{perm(__float_as_uint(xv[1]), __float_as_uint(xv[0]), 0x07060302u), perm(__float_as_uint(xv[3]), __float_as_uint(xv[2]), 0x07060302u),
                                           perm(__float_as_uint(xv[5]), __float_as_uint(xv[4]), 0x07060302u), perm(__float_as_uint(xv[7]), __float_as_uint(xv[6]), 0x07060302u)});
      CFENCE();
      u32x4 yf[8];
      const unsigned ya = L_XN + 4096 + (idx & 7) * 112 + grp * 16;
#pragma unroll
      for (int t = 0; t < 4; ++t) { yf[2 * t] = ld128(n == (unsigned)t ? ya : L_ZERO); yf[2 * t + 1] = ld128(n == (unsigned)t && grp < 3 ? ya + 64 : L_ZERO); }
      unsigned ha[4], hb[4];
#pragma unroll
      for (int t = 0; t < 4; ++t) {
        ha[t] = L_W1 + (8 * grp + q) * 128 + ((t ^ (q >> 1)) << 5) + p * 8; PIN(ha[t]);
        hb[t] = ha[t] + (grp < 3 ? 4096 : 0); PIN(hb[t]);
      }
      unsigned hs = (n < 4 ? 0u : OOB) | (WS_H + idx * 256 + (4 * n + grp) * 16);
      unsigned ho = WS_H + ((idx & ~7u) + (n >> 1)) * 256 + (idx & 7) * 32 + (lane & 1) * 16;
      PIN(hs); PIN(ho);
      if ((WARM & 1) && W1POS) { unsigned d = __builtin_amdgcn_raw_buffer_load_b32(rws, ho, 0, AUX_SC1); PIN(d); }
#pragma unroll
      for (int t = 0; t < 8; ++t) PIN(yf[t]);
      STAMP(1);
      if (W1POS) { wait_ge<4>(L_CNT, 76); __builtin_amdgcn_s_setprio(CPRIO); }
      wait_ge<0>(L_CNT, C_W1);
      STAMP(2);
      f32x4 h0 = {0.f, 0.f, 0.f, 0.f}, h1 = h0;
      u32x4 wf[8];
#pragma unroll
      for (int t = 0; t < 4; ++t) { wf[2 * t] = ldtr2(ha[t], (ha[t] ^ 64) + 512); wf[2 * t + 1] = ldtr2(hb[t], (hb[t] ^ 64) + 512); }
#pragma unroll
      for (int t = 0; t < 4; ++t) { h0 = mfma(wf[2 * t], yf[2 * t], h0); h1 = mfma(wf[2 * t + 1], yf[2 * t + 1], h1); }
      BS128(pack24(h0 + h1, tag), rws, hs, AUX_SC1);
      STAMP(3);
      if (W1POS) __builtin_amdgcn_s_sleep(OSL0); else wait_ge<4>(L_CNT, C_K + HSTART);
      u32x4 hv;
      const unsigned cap = poll_g<OSL>(rws, ho, tt, hv);
      SPINS(SPIN_CAP - cap);
      STAMP(4);
      float hh[4];
#pragma unroll
      for (int k = 0; k < 4; ++k) {
        float s = (k & 1) ? odd24(hv[k - 1], hv[k]) : __uint_as_float(hv[k]);
        s += dpp<ROR2>(s); s += dpp<ROR4>(s); s += dpp<ROR8>(s);
        const float z = rbf(rbf(s) + ((k & 1) ? bhi(bb[k >> 1]) : blo(bb[k >> 1])));
        const float inner = 0.7978845608028654f * (z + 0.044715f * z * z * z);
        const float e = __builtin_amdgcn_exp2f(inner * (2.f * 1.4426950408889634f));
        const float th = 1.f - 2.f * __builtin_amdgcn_rcpf(e + 1.f);
        hh[k] = 0.5f * z * (1.f + th);
      }
      st64(L_OB + (lane & 1) * 16 + 8, u32x2{cvtpk(hh[0], hh[1]), cvtpk(hh[2], hh[3])});
      CFENCE();
      st32(L_HF, 1u);
      STAMP(5);
    }
    LEAVE(5);
  }
#define LD(NT, r, so, va, off) "buffer_load_dwordx4 " va ", " r ", " so " offen offset:" off " " NT " lds\n\t.set fd_n, fd_n + 1\n\t"
#define G4(NT, r, so, a0, a1, a2, a3) "s_nop 0\n\t" LD(NT, r, so, a0, "0") LD(NT, r, so, a1, "1024") LD(NT, r, so, a2, "2048") LD(NT, r, so, a3, "3072")
#define G4N(NT, r, so, a0, a1, a2, a3) LD(NT, r, so, a0, "0") LD(NT, r, so, a1, "1024") LD(NT, r, so, a2, "2048") LD(NT, r, so, a3, "3072")
#define M0N "s_add_u32 m0, m0, 0x1000\n\t"
#define PUB(w) ".rept 16\n\t.if (fd_n - fd_p) > (" w " - 4)\n\t.if fd_p == 0\n\ts_waitcnt vmcnt(fd_n - 2)\n\tv_mov_b32 v17, 2\n\tds_write_b32 v16, v17\n\t.endif\n\t" \
      ".set fd_p, fd_p + 4\n\tv_mov_b32 v17, fd_p\n\ts_waitcnt vmcnt(fd_n - fd_p)\n\tds_write_b32 v16, v17\n\t.endif\n\t.endr\n\t"
#define KV(r, w) ".rept 4\n\t" G4N("nt", r, "s59", "v8", "v9", "v10", "v11") M0N "s_add_u32 s59, s59, 0x1000\n\t" PUB(w) G4N("nt", r, "s59", "v12", "v13", "v14", "v15") M0N "s_add_u32 s59, s59, 0x1000\n\t" PUB(w) ".endr\n\t"
#define W1(w) "s_mov_b32 m0, %[l1]\n\t" G4(NTO, "s[80:83]", "s58", "v4", "v5", "v6", "v7") M0N "s_add_u32 s58, s58, 0x1c000\n\t" PUB(w) \
      "s_nop 0\n\t" LD(NTO, "s[80:83]", "s58", "v4", "0") LD(NTO, "s[80:83]", "s58", "v5", "1024") LD(NTO, "s[80:83]", "s58", "v6", "2048") \
      "s_mov_b32 m0, %[lo]\n\ts_nop 0\n\t" LD(NTO, "s[84:87]", "s63", "v18", "0") PUB(w)
#define LST(i)
#define LEND "s_endpgm\n"
  asm volatile(
            "s_load_dwordx2 s[64:65], %[ka], 0\n\ts_load_dwordx2 s[68:69], %[ka], 8\n\ts_load_dwordx2 s[92:93], %[ka], 56\n\ts_load_dwordx2 s[72:73], %[ka], 16\n\ts_load_dwordx2 s[76:77], %[ka], 24\n\t"
      "s_setprio %c[lp]\n\t.set fd_n, 0\n\t.set fd_p, 0\n\t"
      "s_lshr_b32 s57, %[b], 3\n\ts_and_b32 s56, %[b], 7\n\t"
      "s_lshl_b32 s58, s56, 10\n\ts_lshr_b32 s59, s57, 2\n\ts_lshl_b32 s59, s59, 7\n\ts_add_u32 s58, s58, s59\n\ts_and_b32 s59, s57, 3\n\ts_mul_i32 s59, s59, 0xe0000\n\ts_add_u32 s58, s58, s59\n\t"
      "s_movk_i32 s66, 896\n\ts_mov_b32 s67, 0x20000\n\ts_mov_b32 s70, %[qb]\n\ts_mov_b32 s71, s67\n\ts_movk_i32 s94, 2048\n\ts_mov_b32 s95, s67\n\t"
      "v_lshlrev_b32 v1, 4, %[t]\n\tv_lshlrev_b32 v2, 1, %[t]\n\tv_and_b32 v3, 0x70, v1\n\tv_and_b32 v2, 0x60, v2\n\tv_xor_b32 v18, v1, v2\n\tv_xor_b32 v3, v3, v2\n\tv_lshrrev_b32 v2, 3, %[t]\n\t"
      "v_lshl_add_u32 v4, v2, 13, v3\n\tv_add_u32 v5, 0xfc00, v4\n\tv_add_u32 v6, 0xfc00, v5\n\tv_add_u32 v7, 0xfc00, v6\n\t"
      "v_lshlrev_b32 v16, 2, %[t]\n\tv_add_u32 v16, %[lc], v16\n\t"
      "s_mov_b32 m0, %[lx]\n\ts_waitcnt lgkmcnt(0)\n\t" LST(5)
      LD("", "s[64:67]", "0", "v1", "0")
      "buffer_load_dword off, s[92:95], 0 offset:1024 sc1 lds\n\t.set fd_n, fd_n + 1\n\t"
      "s_mov_b32 m0, 0\n\t"
      ".rept 3\n\t" G4(NTQ, "s[68:71]", "s58", "v4", "v5", "v6", "v7") M0N "s_add_u32 s58, s58, 0x40000\n\t.endr\n\t"
      "s_nop 0\n\t" LD(NTQ, "s[68:71]", "s58", "v4", "0") LD(NTQ, "s[68:71]", "s58", "v5", "1024") LST(1)
      "s_load_dwordx2 s[80:81], %[ka], 32\n\ts_load_dwordx2 s[84:85], %[ka], 40\n\ts_load_dwordx2 s[88:89], %[ka], 48\n\t"
      "s_mov_b32 s74, 0x2000000\n\ts_mov_b32 s75, s67\n\ts_mov_b32 s78, s74\n\ts_mov_b32 s79, s67\n\t"
      "s_lshl_b32 s60, s56, 22\n\ts_add_u32 s60, s60, 0x300000\n\ts_lshl_b32 s59, s57, 15\n\ts_add_u32 s59, s59, s60\n\ts_mov_b32 s60, s59\n\t"
      "v_lshrrev_b32 v9, 1, %[t]\n\tv_and_b32 v9, 16, v9\n\tv_xor_b32 v8, v1, v9\n\tv_xor_b32 v9, 32, v8\n\tv_xor_b32 v10, 64, v8\n\tv_xor_b32 v11, 0x60, v8\n\t"
      "v_xor_b32 v12, 0x80, v8\n\tv_xor_b32 v13, 0xa0, v8\n\tv_xor_b32 v14, 0xc0, v8\n\tv_xor_b32 v15, 0xe0, v8\n\t"
      "s_mul_i32 s61, s56, 28\n\ts_add_u32 s61, s61, s57\n\ts_lshr_b32 s58, s61, 3\n\ts_lshl_b32 s58, s58, 7\n\ts_and_b32 s63, s61, 7\n\ts_mul_i32 s63, s63, 0x31000\n\ts_add_u32 s58, s58, s63\n\t"
      "s_cmp_lt_u32 s57, 28\n\ts_cselect_b32 s82, %[fb], 0\n\ts_mov_b32 s83, s67\n\ts_mov_b32 s90, s82\n\ts_mov_b32 s91, s67\n\ts_mov_b32 s86, s70\n\ts_mov_b32 s87, s67\n\t"
      "s_movk_i32 s62, 0xe00\n\tv_mad_u32_u24 v4, v2, s62, v3\n\tv_add_u32 v5, 0x6c00, v4\n\tv_add_u32 v6, 0x6c00, v5\n\tv_add_u32 v7, 0x6c00, v6\n\t"
      "s_lshl_b32 s63, s56, 5\n\ts_add_u32 s63, s63, s57\n\ts_mul_i32 s63, s63, 0x3800\n\ts_mul_i32 s61, s61, 0x1c00\n\ts_waitcnt lgkmcnt(0)\n\t"
      PUB("%c[wa]")
      W1("%c[wa]")
      "s_mov_b32 m0, %[lk]\n\ts_nop 0\n\t"
      KV("s[72:75]", "%c[wa]") LST(2)
      "s_mov_b32 s59, s60\n\t"
      KV("s[76:79]", "%c[wb]") LST(3)
      "s_mov_b32 m0, %[lo] + 0x400\n\ts_add_u32 s59, s63, 0x400\n\t"
      ".rept 3\n\t" G4N(NTO, "s[84:87]", "s59", "v18", "v18", "v18", "v18") M0N "s_add_u32 s59, s59, 0x1000\n\t" PUB("%c[wc]") ".endr\n\t"
      "s_nop 0\n\t" LD(NTO, "s[84:87]", "s59", "v18", "0") "s_add_u32 m0, m0, 0x400\n\ts_nop 0\n\t"
      LD(NTO, "s[88:91]", "s61", "v18", "0") LD(NTO, "s[88:91]", "s61", "v18", "1024") LD(NTO, "s[88:91]", "s61", "v18", "2048") "s_add_u32 m0, m0, 0xc00\n\ts_add_u32 s61, s61, 0xc00\n\t" PUB("%c[wc]")
      G4(NTO, "s[88:91]", "s61", "v18", "v18", "v18", "v18") LST(6)
      PUB("4") PUB("4") LST(4) LEND
      :
      : [b] "s"(b), [t] "v"(threadIdx.x), [ka] "s"(ap), [fb] "i"(448u * 3584u), [qb] "i"(448u * 8192u),
        [lp] "i"(LPRIO), [lc] "i"(L_CNT), [lx] "i"(L_X), [lo] "i"(L_WO), [lk] "i"(L_K), [l1] "i"(L_W1), [wa] "i"(WA), [wb] "i"(WB), [wc] "i"(WC)
      : "memory", "scc", "s56", "s57", "s58", "s59", "s60", "s61", "s62", "s63", "s64", "s65", "s66", "s67", "s68", "s69", "s70", "s71", "s72", "s73", "s74", "s75", "s76", "s77", "s78", "s79",
        "s80", "s81", "s82", "s83", "s84", "s85", "s86", "s87", "s88", "s89", "s90", "s91", "s92", "s93", "s94", "s95", "s96",
        "v1", "v2", "v3", "v4", "v5", "v6", "v7", "v8", "v9", "v10", "v11", "v12", "v13", "v14", "v15", "v16", "v17", "v18");
  FINISH();
}

extern "C" void kernel_launch(void* const* d_in, const int* in_sizes, int n_in, void* d_out, int out_size, void* d_ws, size_t ws_size, hipStream_t stream) {
  Args a;
  a.x = (const u16*)d_in[0]; a.kc = (const u16*)d_in[1]; a.vc = (const u16*)d_in[2]; a.wq = (const u16*)d_in[3]; a.wo = (const u16*)d_in[4];
  a.w1 = (const u16*)d_in[6]; a.b1 = (const u16*)d_in[7]; a.w2 = (const u16*)d_in[8]; a.b2 = (const u16*)d_in[9];
  a.out = (u16*)d_out; a.ws = (unsigned char*)d_ws;
  fused_decode<<<NWG, NTHR, 0, stream>>>(a);
}
```
